```python
import torch

def _softmax(x, axis=-1):
    return torch.softmax(x, dim=axis)

def _gelu(x):
    return torch.nn.functional.gelu(x, approximate='tanh')

def _silu(x):
    return torch.nn.functional.silu(x)

def _sigmoid(x):
    return torch.sigmoid(x)

def _layernorm(x, axis=-1, eps=1e-05):
    m = x.mean(dim=axis, keepdim=True)
    v = x.var(dim=axis, keepdim=True, unbiased=False)
    return (x - m) / torch.sqrt(v + eps)

def _scatter_add(tgt, idx, src, axis=0):
    return tgt.clone().index_add_(axis, idx, src)

def _segment_sum(src, seg, n):
    out = torch.zeros((n,) + src.shape[1:], dtype=src.dtype)
    return out.index_add_(0, seg, src)

def _pad(x, pw):
    flat = tuple((v for pair in reversed(pw) for v in pair))
    return torch.nn.functional.pad(x, flat)
kernel_name = 'adaLN_cond__sliding_block_cached__swiglu_ffn__prompt'

def setup_inputs():

    _w = lambda a: (lambda t: t.to(torch.bfloat16) if t.is_floating_point() else t)(torch.as_tensor(a))

    def _A_setup():
        B, Sq, D = (1, 2048, 640)
        Hq, Hkv, Sk, Dh = (32, 32, 2048, 128)
        assert Hq % Hkv == 0, (Hq, Hkv)
        return {'x': _w((lambda *s: torch.randn(*s, dtype=torch.bfloat16) * 0.05)(B, Sq, D)), 'k_cache': _w((lambda *s: torch.randn(*s, dtype=torch.bfloat16) * 3.2)(B, Hkv, Sk, Dh)), 'v_cache': _w((lambda *s: torch.randn(*s, dtype=torch.bfloat16) * 0.4)(B, Hkv, Sk, Dh)), 'wq': _w((lambda *s: torch.randn(*s, dtype=torch.bfloat16) * 3.2)(D, Hq * Dh) * D ** (-0.5)), 'wo': _w((lambda *s: torch.randn(*s, dtype=torch.bfloat16) * 0.4)(Hq * Dh, D) * (Hq * Dh) ** (-0.5))}
    _A_in = _A_setup()

    def _B_setup():
        B, S, D, F = (1, 2048, 640, 1728)
        return {'x': _w((lambda *s: torch.randn(*s, dtype=torch.bfloat16) * 0.05)(B, S, D)), 'w_gate': _w((lambda *s: torch.randn(*s, dtype=torch.bfloat16) * 0.05)(D, F)), 'w_up': _w((lambda *s: torch.randn(*s, dtype=torch.bfloat16) * 0.05)(D, F)), 'w_down': _w((lambda *s: torch.randn(*s, dtype=torch.bfloat16) * 0.05)(F, D))}
    _B_in = _B_setup()
    return {'A_x': _A_in['x'], 'A_k_cache': _A_in['k_cache'], 'A_v_cache': _A_in['v_cache'], 'A_wq': _A_in['wq'], 'A_wo': _A_in['wo'], 'B_x': _B_in['x'], 'B_w_gate': _B_in['w_gate'], 'B_w_up': _B_in['w_up'], 'B_w_down': _B_in['w_down'], 'c': _w((lambda *s: torch.randn(*s, dtype=torch.bfloat16) * 0.1)(1, 640) * 8.0), 'w_mod': _w((lambda *s: torch.randn(*s, dtype=torch.bfloat16) * 0.1)(640, 6 * 640) * 640 ** (-0.5) * 8.0)}

def reference(A_x, A_k_cache, A_v_cache, A_wq, A_wo, B_x, B_w_gate, B_w_up, B_w_down, c, w_mod):

    def _A_ref(x, k_cache, v_cache, wq, wo):
        B, Sq, D = x.shape
        Hq, Dh = (32, 128)
        Hkv, Sk = (k_cache.shape[1], k_cache.shape[2])
        W = min(4096, max(256, Sk // 4))
        rep = Hq // Hkv
        q = (lambda x, *d: x.permute(*d))((x @ wq).reshape(B, Sq, Hq, Dh), 0, 2, 1, 3)
        k = (lambda x, n, axis: torch.repeat_interleave(x, n, dim=axis))(k_cache[:, :, Sk - W:, :], rep, axis=1)
        v = (lambda x, n, axis: torch.repeat_interleave(x, n, dim=axis))(v_cache[:, :, Sk - W:, :], rep, axis=1)
        scores = torch.einsum('bhqd,bhkd->bhqk', q, k) * Dh ** (-0.5)
        p = _softmax(scores, axis=-1)
        o = torch.einsum('bhqk,bhkd->bhqd', p, v)
        return (lambda x, *d: x.permute(*d))(o, 0, 2, 1, 3).reshape(B, Sq, Hq * Dh) @ wo

    def _B_ref(x, w_gate, w_up, w_down):
        g = _silu(x @ w_gate)
        u = x @ w_up
        return g * u @ w_down
    x = A_x
    mod = (_silu(c) @ w_mod).reshape(c.shape[0], 6, 1, -1)
    sa, ca, ga, sb, cb, gb = (mod[:, 0].clone(), mod[:, 1].clone(), mod[:, 2].clone(), mod[:, 3].clone(), mod[:, 4].clone(), mod[:, 5].clone())
    h = _layernorm(x, axis=-1) * (1 + ca) + sa
    x = x + ga * _A_ref(h, A_k_cache, A_v_cache, A_wq, A_wo)
    h = _layernorm(x, axis=-1) * (1 + cb) + sb
    x = x + gb * _B_ref(h, B_w_gate, B_w_up, B_w_down)
    return x
```

```cpp
#include <hip/hip_runtime.h>
#include <hip/hip_bf16.h>
#include <stdint.h>

typedef __bf16   bf16x8 __attribute__((ext_vector_type(8)));
typedef __bf16   bf16x4 __attribute__((ext_vector_type(4)));
typedef __bf16   bf16x2 __attribute__((ext_vector_type(2)));
typedef short    s16x4  __attribute__((ext_vector_type(4)));
typedef float    f32x16 __attribute__((ext_vector_type(16)));
typedef float    f32x4  __attribute__((ext_vector_type(4)));
typedef float    f32x2  __attribute__((ext_vector_type(2)));
typedef unsigned u32x4  __attribute__((ext_vector_type(4)));
typedef unsigned u32x2  __attribute__((ext_vector_type(2)));
typedef unsigned short u16;
#define LAS __attribute__((address_space(3)))
#define DI __device__ __forceinline__
#define SBAR() __builtin_amdgcn_sched_barrier(0)
#define MFMA32(a, b, c) __builtin_amdgcn_mfma_f32_32x32x16_bf16((a), (b), (c), 0, 0, 0)
#define MFMA16(a, b, c) __builtin_amdgcn_mfma_f32_16x16x32_bf16((a), (b), (c), 0, 0, 0)

constexpr int S = 2048, D = 640, NH = 32, DH = 128, SK = 2048, W = 512, F = 1728, DQ = NH * DH;
constexpr float EPS = 1e-5f;

constexpr size_t WS_MOD  = 0;
constexpr size_t WS_CSBS = 16384;
constexpr size_t WS_STAT = WS_CSBS + 27 * 1024 + 4096;
constexpr size_t WS_H    = 262144;
constexpr size_t WS_X1   = WS_H + (size_t)S * D * 2;
constexpr size_t WS_Y    = WS_X1 + (size_t)S * D * 2;
constexpr size_t WS_O    = WS_Y + (size_t)S * D * 2;
constexpr size_t WS_ACT  = WS_O + (size_t)S * DQ * 2;
constexpr size_t WS_WOT  = WS_ACT + (size_t)S * F * 2;
constexpr size_t WS_WDT  = WS_WOT + (size_t)DQ * D * 2;
constexpr size_t WS_WGU  = WS_WDT + (size_t)F * D * 2;
constexpr size_t WS_END  = WS_WGU + (size_t)D * F * 2 * 2;
static_assert(WS_STAT + (size_t)S * 64 <= WS_H, "ws map");

DI float bf2f(u16 v) { return __uint_as_float((unsigned)v << 16); }
DI float bflo(unsigned v) { return __uint_as_float(v << 16); }
DI float bfhi(unsigned v) { return __uint_as_float(v & 0xffff0000u); }
DI unsigned pk2(float lo, float hi) { f32x2 v = {lo, hi}; bf16x2 b = __builtin_convertvector(v, bf16x2); return __builtin_bit_cast(unsigned, b); }
DI float rbf(float x) { return bflo(pk2(x, 0.f)); }
DI unsigned rfl(unsigned x) { return __builtin_amdgcn_readfirstlane(x); }
DI int swap23(int i) { return (i & ~12) | ((i & 4) << 1) | ((i & 8) >> 1); }

DI void dma16(unsigned m0v, unsigned voff, __amdgpu_buffer_rsrc_t srd, unsigned soff) {
  asm volatile("s_mov_b32 m0, %0\n\ts_nop 0\n\tbuffer_load_dwordx4 %1, %2, %3 offen lds" :: "s"(m0v), "v"(voff), "s"(srd), "s"(soff) : "memory");
}
template <int OFF> DI void ld16(bf16x8& r, unsigned voff, __amdgpu_buffer_rsrc_t srd, unsigned soff) {
  asm volatile("buffer_load_dwordx4 %0, %1, %2, %3 offen offset:%4" : "=v"(r) : "v"(voff), "s"(srd), "s"(soff), "i"(OFF) : "memory");
}
template <int N> DI void wait_bar4(bf16x8& a, bf16x8& b, bf16x8& c, bf16x8& d) {
  asm volatile("s_waitcnt vmcnt(%4) lgkmcnt(0)\n\ts_barrier" : "+v"(a), "+v"(b), "+v"(c), "+v"(d) : "i"(N) : "memory");
}
template <int N> DI void wait_bar() { asm volatile("s_waitcnt vmcnt(%0) lgkmcnt(0)\n\ts_barrier" :: "i"(N) : "memory"); }
DI bf16x8 lds_rd(const LAS char* p) { return *(const LAS bf16x8*)p; }
DI bf16x8 lds_tr(const LAS char* p0, const LAS char* p1) {
  s16x4 lo = __builtin_amdgcn_ds_read_tr16_b64_v4i16((LAS s16x4*)p0);
  s16x4 hi = __builtin_amdgcn_ds_read_tr16_b64_v4i16((LAS s16x4*)p1);
  return __builtin_bit_cast(bf16x8, __builtin_shufflevector(lo, hi, 0, 1, 2, 3, 4, 5, 6, 7));
}
DI bf16x8 pack8(const f32x16& x, int s, float m) {
  u32x4 w = {pk2(x[8 * s] * m, x[8 * s + 1] * m), pk2(x[8 * s + 2] * m, x[8 * s + 3] * m), pk2(x[8 * s + 4] * m, x[8 * s + 5] * m), pk2(x[8 * s + 6] * m, x[8 * s + 7] * m)};
  return __builtin_bit_cast(bf16x8, w);
}
DI __amdgpu_buffer_rsrc_t mk_srd(const void* p, unsigned bytes) { return __builtin_amdgcn_make_buffer_rsrc((void*)p, (short)0, (int)bytes, 0x00020000); }

constexpr int K0_MODB = 120;
constexpr int NCH_WO = DQ * D / 8, NCH_WD = F * D / 8, NCH_GU = D * F * 2 / 8;
constexpr int K0_TILEB = (NCH_WO + NCH_WD + NCH_GU) / 256;
__global__ __launch_bounds__(256) void k0_mod_tile(const u16* __restrict__ c, const u16* __restrict__ wmod, const u16* __restrict__ wo,
                                                   const u16* __restrict__ wg, const u16* __restrict__ wu, const u16* __restrict__ wd, char* __restrict__ ws) {
  __shared__ float red[4][32];
  const int tid = threadIdx.x, bid = blockIdx.x;
  if (bid < K0_MODB) {
    const int rg = tid >> 2, ch = tid & 3;
    float acc[8] = {0, 0, 0, 0, 0, 0, 0, 0};
    u32x4 wv[10]; float sc[10];
#pragma unroll
    for (int i = 0; i < 10; ++i) wv[i] = *(const u32x4*)(wmod + (size_t)(rg + 64 * i) * (6 * D) + bid * 32 + ch * 8);
#pragma unroll
    for (int i = 0; i < 10; ++i) { float cv = bf2f(c[rg + 64 * i]); sc[i] = rbf(cv / (1.f + __expf(-cv))); }
#pragma unroll
    for (int i = 0; i < 10; ++i)
#pragma unroll
      for (int e = 0; e < 4; ++e) { acc[2 * e] += sc[i] * bflo(wv[i][e]); acc[2 * e + 1] += sc[i] * bfhi(wv[i][e]); }
#pragma unroll
    for (int e = 0; e < 8; ++e)
#pragma unroll
      for (int o = 4; o < 64; o <<= 1) acc[e] += __shfl_xor(acc[e], o);
    if ((tid & 63) < 4)
#pragma unroll
      for (int e = 0; e < 8; ++e) red[tid >> 6][ch * 8 + e] = acc[e];
    __syncthreads();
    if (tid < 32) ((float*)(ws + WS_MOD))[bid * 32 + tid] = rbf(red[0][tid] + red[1][tid] + red[2][tid] + red[3][tid]);
    return;
  }
  int id = (bid - K0_MODB) * 256 + tid;
  const u16* src; int stride; char* dst;
  if (id < NCH_WO + NCH_WD) {
    const bool isO = id < NCH_WO; if (!isO) id -= NCH_WO;
    const int KS = isO ? 128 : 54;
    const int lane = id & 63, fr = id >> 6, c5 = fr % 5, t = fr / 5, ks = t % KS, nt = t / KS;
    src = (isO ? wo : wd) + (size_t)(32 * ks + 8 * (lane >> 4)) * D + 80 * nt + 16 * c5 + (lane & 15); stride = D;
    dst = ws + (isO ? WS_WOT : WS_WDT) + (size_t)id * 16;
  } else {
    id -= NCH_WO + NCH_WD;
    const int lane = id & 63, fr = (id >> 6) & 3, t = id >> 8, ks = t % 40, j = t / 40;
    src = ((fr & 2) ? wu : wg) + (size_t)(16 * ks + 8 * (lane >> 5)) * F + 64 * j + 32 * (fr & 1) + swap23(lane & 31); stride = F;
    dst = ws + WS_WGU + (size_t)id * 16;
  }
  u16 v[8];
#pragma unroll
  for (int j = 0; j < 8; ++j) v[j] = src[(size_t)j * stride];
  u32x4 o = {v[0] | ((unsigned)v[1] << 16), v[2] | ((unsigned)v[3] << 16), v[4] | ((unsigned)v[5] << 16), v[6] | ((unsigned)v[7] << 16)};
  *(u32x4*)dst = o;
}

__global__ __launch_bounds__(256) void k1_ln(const u16* __restrict__ x, const u16* __restrict__ wg, const u16* __restrict__ wu, char* __restrict__ ws) {
  __shared__ float red[4][128];
  const int tid = threadIdx.x, bid = blockIdx.x, lane = tid & 63, wv = tid >> 6;
  const float* mod = (const float*)(ws + WS_MOD);
  if (bid < 64) {
    const int row = bid * 32 + wv * 8 + (lane & 7), cg = lane >> 3;
    u32x4 xv[10];
#pragma unroll
    for (int i = 0; i < 10; ++i) xv[i] = *(const u32x4*)(x + (size_t)row * D + (cg + 8 * i) * 8);
    float s = 0.f;
#pragma unroll
    for (int i = 0; i < 10; ++i)
#pragma unroll
      for (int e = 0; e < 4; ++e) s += bflo(xv[i][e]) + bfhi(xv[i][e]);
    s += __shfl_xor(s, 8); s += __shfl_xor(s, 16); s += __shfl_xor(s, 32);
    const float m = s * (1.f / D);
    float q = 0.f;
#pragma unroll
    for (int i = 0; i < 10; ++i)
#pragma unroll
      for (int e = 0; e < 4; ++e) { float a = bflo(xv[i][e]) - m, b = bfhi(xv[i][e]) - m; q += a * a + b * b; }
    q += __shfl_xor(q, 8); q += __shfl_xor(q, 16); q += __shfl_xor(q, 32);
    const float r = rsqrtf(q * (1.f / D) + EPS);
#pragma unroll
    for (int i = 0; i < 10; ++i) {
      const int ch = cg + 8 * i;
      const f32x4 s0 = *(const f32x4*)(mod + ch * 8), s1 = *(const f32x4*)(mod + ch * 8 + 4);
      const f32x4 c0 = *(const f32x4*)(mod + D + ch * 8), c1 = *(const f32x4*)(mod + D + ch * 8 + 4);
      u32x4 o;
      o[0] = pk2((bflo(xv[i][0]) - m) * r * rbf(1.f + c0[0]) + s0[0], (bfhi(xv[i][0]) - m) * r * rbf(1.f + c0[1]) + s0[1]);
      o[1] = pk2((bflo(xv[i][1]) - m) * r * rbf(1.f + c0[2]) + s0[2], (bfhi(xv[i][1]) - m) * r * rbf(1.f + c0[3]) + s0[3]);
      o[2] = pk2((bflo(xv[i][2]) - m) * r * rbf(1.f + c1[0]) + s1[0], (bfhi(xv[i][2]) - m) * r * rbf(1.f + c1[1]) + s1[1]);
      o[3] = pk2((bflo(xv[i][3]) - m) * r * rbf(1.f + c1[2]) + s1[2], (bfhi(xv[i][3]) - m) * r * rbf(1.f + c1[3]) + s1[3]);
      *(u32x4*)(ws + WS_H + ((size_t)(row >> 5) * 40 + (ch >> 1)) * 1024 + ((row & 31) + 32 * (ch & 1)) * 16) = o;
    }
    return;
  }
  const int b = bid - 64, j = b >> 1, which = b & 1;
  const u16* wsrc = (which ? wu : wg) + 64 * j;
  const int rg = tid >> 3, ch = tid & 7;
  float ac[8] = {0, 0, 0, 0, 0, 0, 0, 0}, ab[8] = {0, 0, 0, 0, 0, 0, 0, 0};
#pragma unroll 4
  for (int i = 0; i < 20; ++i) {
    const int k = rg + 32 * i;
    const u32x4 w4 = *(const u32x4*)(wsrc + (size_t)k * F + ch * 8);
    const float a = rbf(1.f + mod[4 * D + k]), sb = mod[3 * D + k];
#pragma unroll
    for (int e = 0; e < 4; ++e) { const float lo = bflo(w4[e]), hi = bfhi(w4[e]); ac[2 * e] += a * lo; ac[2 * e + 1] += a * hi; ab[2 * e] += sb * lo; ab[2 * e + 1] += sb * hi; }
  }
#pragma unroll
  for (int e = 0; e < 8; ++e)
#pragma unroll
    for (int o = 8; o < 64; o <<= 1) { ac[e] += __shfl_xor(ac[e], o); ab[e] += __shfl_xor(ab[e], o); }
  if (lane < 8)
#pragma unroll
    for (int e = 0; e < 8; ++e) { red[wv][ch * 8 + e] = ac[e]; red[wv][64 + ch * 8 + e] = ab[e]; }
  __syncthreads();
  if (tid < 128) ((float*)(ws + WS_CSBS))[j * 256 + which * 128 + tid] = red[0][tid] + red[1][tid] + red[2][tid] + red[3][tid];
}

constexpr int SLOT = 16384, NSLOT = 8;
constexpr float QSCALE = 0.08838834764831845f * 1.4426950408889634f;
constexpr float RTHR = 30.f;

struct K2C {
  __amdgpu_buffer_rsrc_t srdW, srdK, srdV, srdH;
  unsigned voffW, voffK, voffV, voffH, m0base, sofW, sofKV, sofH;
  const LAS char *pW, *pK0, *pK1, *pV;
};
template <int I> DI void k2_dma(const K2C& c) {
  if constexpr (I < 26) {
    const unsigned m0v = c.m0base + (I & (NSLOT - 1)) * SLOT;
    if constexpr (I < 10) { dma16(m0v, c.voffW, c.srdW, c.sofW + I * 64 * 8192); dma16(m0v + 1024, c.voffW, c.srdW, c.sofW + I * 64 * 8192 + 128); }
    else if constexpr (((I - 10) & 1) == 0) { dma16(m0v, c.voffK, c.srdK, c.sofKV + ((I - 10) >> 1) * 16384); dma16(m0v + 1024, c.voffK, c.srdK, c.sofKV + ((I - 10) >> 1) * 16384 + 128); }
    else { dma16(m0v, c.voffV, c.srdV, c.sofKV + ((I - 10) >> 1) * 16384); dma16(m0v + 1024, c.voffV, c.srdV, c.sofKV + ((I - 10) >> 1) * 16384 + 128); }
  }
}
template <int I> DI void k2_ldh(const K2C& c, bf16x8 (&hf)[4]) {
  ld16<0>(hf[0], c.voffH, c.srdH, c.sofH + I * 4096); ld16<1024>(hf[1], c.voffH, c.srdH, c.sofH + I * 4096);
  ld16<2048>(hf[2], c.voffH, c.srdH, c.sofH + I * 4096); ld16<3072>(hf[3], c.voffH, c.srdH, c.sofH + I * 4096);
}
constexpr int qp_wait(int i) { return i <= 4 ? 32 : (i == 5 ? 28 : 48 - 4 * i); }
template <int I> DI void k2_qproj(const K2C& c, bf16x8 (&hf)[10][4], f32x16 (&acc)[4]) {
  wait_bar4<qp_wait(I)>(hf[I][0], hf[I][1], hf[I][2], hf[I][3]);
  if constexpr (I + 6 < 10) k2_ldh<I + 6>(c, hf[I + 6]);
  k2_dma<I + 7>(c);
  const LAS char* p = c.pW + (I & (NSLOT - 1)) * SLOT;
#pragma unroll
  for (int s = 0; s < 4; ++s)
#pragma unroll
    for (int dt = 0; dt < 4; ++dt) acc[dt] = MFMA32(lds_tr(p + s * 4096 + dt * 512, p + s * 4096 + dt * 512 + 256), hf[I][s], acc[dt]);
}
#define MX3(a, b, c) __builtin_fmaxf(__builtin_fmaxf((a), (b)), (c))
template <int T> DI void k2_attn(const K2C& c, const bf16x8 (&qf)[8], f32x16 (&o)[4], f32x16& negm, float& mref, float& lsum) {
  constexpr int IK = 10 + 2 * T, IV = IK + 1;
  constexpr int issued = T == 0 ? 16 : (15 + 2 * T > 25 ? 25 : 15 + 2 * T);
  wait_bar<2 * (issued - IV)>();
  if constexpr (T == 0) k2_dma<17>(c); else { k2_dma<16 + 2 * T>(c); k2_dma<17 + 2 * T>(c); }
  const LAS char* pk0 = c.pK0 + (IK & (NSLOT - 1)) * SLOT;
  const LAS char* pk1 = c.pK1 + (IK & (NSLOT - 1)) * SLOT;
  const LAS char* pv = c.pV + (IV & (NSLOT - 1)) * SLOT;
  f32x16 p[2];
#pragma unroll
  for (int kb = 0; kb < 2; ++kb) {
    p[kb] = negm;
#pragma unroll
    for (int s = 0; s < 8; ++s) p[kb] = MFMA32(lds_rd(((s & 1) ? pk1 : pk0) + kb * 8192 + (s >> 1) * 512), qf[s], p[kb]);
  }
  float a = MX3(p[0][0], p[0][1], p[0][2]), b = MX3(p[1][0], p[1][1], p[1][2]);
#pragma unroll
  for (int r = 3; r < 15; r += 2) { a = MX3(a, p[0][r], p[0][r + 1]); b = MX3(b, p[1][r], p[1][r + 1]); }
  a = MX3(a, p[0][15], p[1][15]); a = __builtin_fmaxf(a, b);
  if (T == 0 || __builtin_expect(__any(a > RTHR), 0)) {
    auto rr = __builtin_amdgcn_permlane32_swap(__float_as_uint(a), __float_as_uint(a), false, false);
    float dl = __builtin_fmaxf(__uint_as_float(rr[0]), __uint_as_float(rr[1]));
    if (T != 0) {
      dl = __builtin_fmaxf(dl, 0.f);
      const float f = __builtin_amdgcn_exp2f(-dl);
      lsum *= f;
#pragma unroll
      for (int dt = 0; dt < 4; ++dt) o[dt] *= f;
    }
    mref += dl;
#pragma unroll
    for (int r = 0; r < 16; ++r) { p[0][r] -= dl; p[1][r] -= dl; negm[r] = -mref; }
  }
#pragma unroll
  for (int s = 0; s < 4; ++s) {
    f32x16& pp = p[s >> 1];
#pragma unroll
    for (int j = 0; j < 8; ++j) { const int r = 8 * (s & 1) + j; pp[r] = __builtin_amdgcn_exp2f(pp[r]); lsum += pp[r]; }
    const bf16x8 pf = pack8(pp, s & 1, 1.f);
#pragma unroll
    for (int dt = 0; dt < 4; ++dt) o[dt] = MFMA32(lds_tr(pv + s * 4096 + dt * 512, pv + s * 4096 + 2048 + dt * 512), pf, o[dt]);
  }
}

__global__ __launch_bounds__(512, 2) void k2_attention(const u16* __restrict__ wq, const u16* __restrict__ kc, const u16* __restrict__ vc, char* __restrict__ ws) {
  extern __shared__ __attribute__((aligned(16))) char smem[];
  const int tid = threadIdx.x, lane = tid & 63, bid = blockIdx.x;
  const unsigned w = rfl(tid >> 6);
  const int head = (bid & 7) * 4 + ((bid >> 3) & 3), qb = bid >> 5;
  const int r32 = lane & 31, h = lane >> 5;
  K2C c;
  c.srdW = mk_srd(wq, D * DQ * 2); c.srdK = mk_srd(kc, NH * SK * DH * 2); c.srdV = mk_srd(vc, NH * SK * DH * 2); c.srdH = mk_srd(ws + WS_H, S * D * 2);
  {
    const unsigned row = (lane >> 2) & 7, sub = lane >> 5, ch = lane & 3;
    c.voffW = row * 8192 + sub * 64 + ch * 16;
    c.voffV = row * 256 + sub * 64 + ch * 16;
    c.voffK = row * 256 + sub * 64 + ((ch ^ ((2 * w + (row >> 2)) & 3)) * 16);
    c.voffH = lane * 16;
    c.m0base = (unsigned)(size_t)(LAS char*)smem + w * 2048;
    c.sofW = w * 8 * 8192 + head * 256;
    c.sofKV = (head * SK + (SK - W) + w * 8) * 256;
    c.sofH = (qb * 8 + w) * 40 * 1024;
    const LAS char* sb = (const LAS char*)smem;
    const int blk = (lane >> 4) & 1, q = (lane >> 2) & 3, p = lane & 3, sp = ((p & 1) << 1) | (p >> 1);
    c.pW = sb + h * 2048 + q * 64 + blk * 32 + sp * 8;
    c.pV = sb + (4 * h + q) * 64 + blk * 32 + sp * 8;
    c.pK0 = sb + 2048 * (r32 >> 3) + 64 * (r32 & 7) + 16 * (h ^ ((r32 >> 2) & 3));
    c.pK1 = sb + 2048 * (r32 >> 3) + 64 * (r32 & 7) + 16 * ((2 + h) ^ ((r32 >> 2) & 3));
  }
  asm volatile("s_nop 4" ::: "memory");
  bf16x8 hf[10][4];
  k2_ldh<0>(c, hf[0]); k2_dma<0>(c); k2_ldh<1>(c, hf[1]); k2_dma<1>(c); k2_ldh<2>(c, hf[2]); k2_dma<2>(c);
  k2_ldh<3>(c, hf[3]); k2_dma<3>(c); k2_ldh<4>(c, hf[4]); k2_dma<4>(c); k2_ldh<5>(c, hf[5]); k2_dma<5>(c); k2_dma<6>(c);
  if (w < 4) __builtin_amdgcn_s_setprio(1);
  f32x16 acc[4] = {};
  k2_qproj<0>(c, hf, acc); k2_qproj<1>(c, hf, acc); k2_qproj<2>(c, hf, acc); k2_qproj<3>(c, hf, acc); k2_qproj<4>(c, hf, acc);
  k2_qproj<5>(c, hf, acc); k2_qproj<6>(c, hf, acc); k2_qproj<7>(c, hf, acc); k2_qproj<8>(c, hf, acc); k2_qproj<9>(c, hf, acc);
  bf16x8 qf[8];
#pragma unroll
  for (int s = 0; s < 8; ++s) qf[s] = pack8(acc[s >> 1], s & 1, QSCALE);
  f32x16 o[4] = {}, negm = {};
  float mref = 0.f, lsum = 0.f;
  k2_attn<0>(c, qf, o, negm, mref, lsum); k2_attn<1>(c, qf, o, negm, mref, lsum); k2_attn<2>(c, qf, o, negm, mref, lsum); k2_attn<3>(c, qf, o, negm, mref, lsum);
  k2_attn<4>(c, qf, o, negm, mref, lsum); k2_attn<5>(c, qf, o, negm, mref, lsum); k2_attn<6>(c, qf, o, negm, mref, lsum); k2_attn<7>(c, qf, o, negm, mref, lsum);
  { auto rr = __builtin_amdgcn_permlane32_swap(__float_as_uint(lsum), __float_as_uint(lsum), false, false); lsum = __uint_as_float(rr[0]) + __uint_as_float(rr[1]); }
  const float il = 1.f / lsum;
  const int qg = qb * 256 + w * 32 + r32;
  char* ob = ws + WS_O + ((size_t)((qg >> 6) * 128 + head * 4) * 4 + ((qg >> 4) & 3)) * 1024 + ((qg & 15) + 16 * h) * 16;
#pragma unroll
  for (int dt = 0; dt < 4; ++dt)
#pragma unroll
    for (int s = 0; s < 2; ++s) *(bf16x8*)(ob + dt * 4096 + s * 512) = pack8(o[dt], s, il);
}

constexpr int RLD = 84;
template <int KS, bool FINAL>
__global__ __launch_bounds__(256, 1) void k35_gemm(const char* __restrict__ act, const char* __restrict__ wt, const u16* __restrict__ x, char* __restrict__ ws, u16* __restrict__ out) {
  extern __shared__ __attribute__((aligned(16))) char smem[];
  const int tid = threadIdx.x, lane = tid & 63, bid = blockIdx.x;
  const int w = rfl(tid >> 6);
  const int rb = (bid & 7) * 4 + ((bid >> 3) & 3), nt = bid >> 5;
  constexpr int KW = (KS + 3) / 4;
  constexpr int NFULL = KS - 4 * (KW - 1);
  const int k0 = w < NFULL ? w * KW : NFULL * KW + (w - NFULL) * (KW - 1), nk = w < NFULL ? KW : KW - 1;
  const char* pa = act + ((size_t)rb * KS + k0) * 4096 + lane * 16;
  const char* pw = wt + ((size_t)nt * KS + k0) * 5120 + lane * 16;
  constexpr int PD = 4;
  bf16x8 fa[PD][4], fw[PD][5];
  f32x4 acc[4][5] = {};
#pragma unroll
  for (int i = 0; i < PD; ++i) {
#pragma unroll
    for (int r = 0; r < 4; ++r) fa[i][r] = *(const bf16x8*)(pa + i * 4096 + r * 1024);
#pragma unroll
    for (int cc = 0; cc < 5; ++cc) fw[i][cc] = *(const bf16x8*)(pw + i * 5120 + cc * 1024);
  }
#pragma unroll
  for (int i = 0; i < KW; ++i) {
    const int b = i % PD;
    if (i < KW - 1 || nk == KW) {
#pragma unroll
      for (int r = 0; r < 4; ++r)
#pragma unroll
        for (int cc = 0; cc < 5; ++cc) acc[r][cc] = MFMA16(fw[b][cc], fa[b][r], acc[r][cc]);
    }
    SBAR();
    if (i + PD < KW - 1 || (i + PD == KW - 1 && nk == KW)) {
#pragma unroll
      for (int r = 0; r < 4; ++r) fa[b][r] = *(const bf16x8*)(pa + (i + PD) * 4096 + r * 1024);
#pragma unroll
      for (int cc = 0; cc < 5; ++cc) fw[b][cc] = *(const bf16x8*)(pw + (i + PD) * 5120 + cc * 1024);
    }
    SBAR();
  }
  float* red = (float*)smem;
#pragma unroll
  for (int r = 0; r < 4; ++r)
#pragma unroll
    for (int cc = 0; cc < 5; ++cc) *(f32x4*)(red + (w * 64 + r * 16 + (lane & 15)) * RLD + cc * 16 + (lane >> 4) * 4) = acc[r][cc];
  __syncthreads();
  const float* mod = (const float*)(ws + WS_MOD);
  const int q = lane, row = rb * 64 + q;
  float v[3][8];
#pragma unroll
  for (int it = 0; it < 3; ++it) {
    const int ch = w + 4 * it;
    if (ch < 10) {
      f32x4 s0 = {}, s1 = {};
#pragma unroll
      for (int ww = 0; ww < 4; ++ww) { s0 += *(const f32x4*)(red + (ww * 64 + q) * RLD + ch * 8); s1 += *(const f32x4*)(red + (ww * 64 + q) * RLD + ch * 8 + 4); }
      const int n0 = nt * 80 + ch * 8;
      const size_t toff = ((size_t)(row >> 5) * 40 + (n0 >> 4)) * 1024 + ((row & 31) + 32 * ((n0 >> 3) & 1)) * 16;
      const float* g = mod + (FINAL ? 5 : 2) * D + n0;
      const f32x4 g0 = *(const f32x4*)g, g1 = *(const f32x4*)(g + 4);
      const u32x4 xv = FINAL ? *(const u32x4*)(ws + WS_X1 + toff) : *(const u32x4*)(x + (size_t)row * D + n0);
      u32x4 o;
      o[0] = pk2(bflo(xv[0]) + g0[0] * s0[0], bfhi(xv[0]) + g0[1] * s0[1]); o[1] = pk2(bflo(xv[1]) + g0[2] * s0[2], bfhi(xv[1]) + g0[3] * s0[3]);
      o[2] = pk2(bflo(xv[2]) + g1[0] * s1[0], bfhi(xv[2]) + g1[1] * s1[1]); o[3] = pk2(bflo(xv[3]) + g1[2] * s1[2], bfhi(xv[3]) + g1[3] * s1[3]);
      if (FINAL) { *(u32x4*)(out + (size_t)row * D + n0) = o; }
      else {
        *(u32x4*)(ws + WS_X1 + toff) = o;
        const f32x4 c0 = *(const f32x4*)(mod + 4 * D + n0), c1 = *(const f32x4*)(mod + 4 * D + n0 + 4);
#pragma unroll
        for (int e = 0; e < 4; ++e) { v[it][2 * e] = bflo(o[e]); v[it][2 * e + 1] = bfhi(o[e]); }
        u32x4 y;
        y[0] = pk2(v[it][0] * rbf(1.f + c0[0]), v[it][1] * rbf(1.f + c0[1])); y[1] = pk2(v[it][2] * rbf(1.f + c0[2]), v[it][3] * rbf(1.f + c0[3]));
        y[2] = pk2(v[it][4] * rbf(1.f + c1[0]), v[it][5] * rbf(1.f + c1[1])); y[3] = pk2(v[it][6] * rbf(1.f + c1[2]), v[it][7] * rbf(1.f + c1[3]));
        *(u32x4*)(ws + WS_Y + toff) = y;
      }
    }
  }
  if (!FINAL) {
    const int cnt = w < 2 ? 24 : 16;
    float s = 0.f;
#pragma unroll
    for (int it = 0; it < 3; ++it) if (w + 4 * it < 10)
#pragma unroll
      for (int e = 0; e < 8; ++e) s += v[it][e];
    const float mu = s / cnt; float m2 = 0.f;
#pragma unroll
    for (int it = 0; it < 3; ++it) if (w + 4 * it < 10)
#pragma unroll
      for (int e = 0; e < 8; ++e) { const float d = v[it][e] - mu; m2 += d * d; }
    float* st = red + 4 * 64 * RLD;
    *(f32x2*)(st + (w * 64 + q) * 2) = (f32x2){mu, m2};
    __syncthreads();
    if (w == 0) {
      f32x2 a[4];
#pragma unroll
      for (int ww = 0; ww < 4; ++ww) a[ww] = *(const f32x2*)(st + (ww * 64 + q) * 2);
      const float mt = (24.f * (a[0][0] + a[1][0]) + 16.f * (a[2][0] + a[3][0])) * (1.f / 80.f);
      float M = a[0][1] + a[1][1] + a[2][1] + a[3][1];
      M += 24.f * ((a[0][0] - mt) * (a[0][0] - mt) + (a[1][0] - mt) * (a[1][0] - mt)) + 16.f * ((a[2][0] - mt) * (a[2][0] - mt) + (a[3][0] - mt) * (a[3][0] - mt));
      *(f32x2*)(ws + WS_STAT + (size_t)row * 64 + nt * 8) = (f32x2){mt, M};
    }
  }
}

struct K4C { __amdgpu_buffer_rsrc_t srdW, srdH; unsigned voff, m0base, sofW, sofH; const LAS char* pW; };
template <int I> DI void k4_dma(const K4C& c) {
  if constexpr (I < 10) { const unsigned m0v = c.m0base + (I & (NSLOT - 1)) * SLOT; dma16(m0v, c.voff, c.srdW, c.sofW + I * 16384); dma16(m0v + 1024, c.voff, c.srdW, c.sofW + I * 16384 + 1024); }
}
template <int I> DI void k4_ldh(const K4C& c, bf16x8 (&hf)[4]) {
  ld16<0>(hf[0], c.voff, c.srdH, c.sofH + I * 4096); ld16<1024>(hf[1], c.voff, c.srdH, c.sofH + I * 4096);
  ld16<2048>(hf[2], c.voff, c.srdH, c.sofH + I * 4096); ld16<3072>(hf[3], c.voff, c.srdH, c.sofH + I * 4096);
}
constexpr int k4_wait(int i) { return i == 0 ? 32 : i == 1 ? 32 : i == 2 ? 32 : i == 3 ? 32 : i == 4 ? 30 : i == 5 ? 24 : i == 6 ? 18 : i == 7 ? 12 : i == 8 ? 6 : 0; }
template <int I> DI void k4_step(const K4C& c, bf16x8 (&hf)[10][4], f32x16 (&acc)[4]) {
  wait_bar4<k4_wait(I)>(hf[I][0], hf[I][1], hf[I][2], hf[I][3]);
  if constexpr (I + 6 < 10) k4_ldh<I + 6>(c, hf[I + 6]);
  k4_dma<I + 7>(c);
  const LAS char* p = c.pW + (I & (NSLOT - 1)) * SLOT;
#pragma unroll
  for (int s = 0; s < 4; ++s)
#pragma unroll
    for (int fr = 0; fr < 4; ++fr) acc[fr] = MFMA32(lds_rd(p + (s * 4 + fr) * 1024), hf[I][s], acc[fr]);
}
__global__ __launch_bounds__(512, 2) void k4_gateup(char* __restrict__ ws) {
  extern __shared__ __attribute__((aligned(16))) char smem[];
  const int tid = threadIdx.x, lane = tid & 63, bid = blockIdx.x;
  const unsigned w = rfl(tid >> 6);
  const int qb = bid & 7, j = bid >> 3, r32 = lane & 31, h = lane >> 5;
  const int qg = qb * 256 + w * 32 + r32;
  f32x4 st[4];
#pragma unroll
  for (int i = 0; i < 4; ++i) st[i] = *(const f32x4*)(ws + WS_STAT + (size_t)qg * 64 + i * 16);
  if (tid < 64) *(LAS f32x4*)((LAS char*)smem + NSLOT * SLOT + tid * 16) = *(const f32x4*)(ws + WS_CSBS + j * 1024 + tid * 16);
  float mean = 0.f;
#pragma unroll
  for (int i = 0; i < 4; ++i) mean += st[i][0] + st[i][2];
  mean *= 0.125f;
  float m2 = 0.f;
#pragma unroll
  for (int i = 0; i < 4; ++i) m2 += st[i][1] + st[i][3] + 80.f * ((st[i][0] - mean) * (st[i][0] - mean) + (st[i][2] - mean) * (st[i][2] - mean));
  float rs = rsqrtf(m2 * (1.f / D) + EPS);
  asm volatile("" : "+v"(rs), "+v"(mean));
  K4C c;
  c.srdW = mk_srd(ws + WS_WGU, D * F * 4); c.srdH = mk_srd(ws + WS_Y, S * D * 2);
  c.voff = lane * 16; c.m0base = (unsigned)(size_t)(LAS char*)smem + w * 2048;
  c.sofW = j * 163840 + w * 2048; c.sofH = (qb * 8 + w) * 40 * 1024; c.pW = (const LAS char*)smem + lane * 16;
  asm volatile("s_nop 4" ::: "memory");
  bf16x8 hf[10][4];
  k4_ldh<0>(c, hf[0]); k4_dma<0>(c); k4_ldh<1>(c, hf[1]); k4_dma<1>(c); k4_ldh<2>(c, hf[2]); k4_dma<2>(c);
  k4_ldh<3>(c, hf[3]); k4_dma<3>(c); k4_ldh<4>(c, hf[4]); k4_dma<4>(c); k4_ldh<5>(c, hf[5]); k4_dma<5>(c); k4_dma<6>(c);
  f32x16 acc[4] = {};
  k4_step<0>(c, hf, acc); k4_step<1>(c, hf, acc); k4_step<2>(c, hf, acc); k4_step<3>(c, hf, acc); k4_step<4>(c, hf, acc);
  k4_step<5>(c, hf, acc); k4_step<6>(c, hf, acc); k4_step<7>(c, hf, acc); k4_step<8>(c, hf, acc); k4_step<9>(c, hf, acc);
  const LAS float* cb = (const LAS float*)((LAS char*)smem + NSLOT * SLOT);
  const float nmr = -mean * rs;
  char* ob = ws + WS_ACT + ((size_t)((qg >> 6) * 54 + j * 2) * 4 + ((qg >> 4) & 3)) * 1024 + ((qg & 15) + 16 * h) * 16;
#pragma unroll
  for (int t = 0; t < 2; ++t)
#pragma unroll
    for (int s = 0; s < 2; ++s) {
      const int f0 = 32 * t + 16 * s + 8 * h;
      float a[8];
#pragma unroll
      for (int hh = 0; hh < 2; ++hh) {
        const f32x4 csg = *(const LAS f32x4*)(cb + f0 + 4 * hh), bsg = *(const LAS f32x4*)(cb + 64 + f0 + 4 * hh);
        const f32x4 csu = *(const LAS f32x4*)(cb + 128 + f0 + 4 * hh), bsu = *(const LAS f32x4*)(cb + 192 + f0 + 4 * hh);
#pragma unroll
        for (int e = 0; e < 4; ++e) {
          const int r = 8 * s + 4 * hh + e;
          const float g = rs * acc[t][r] + (nmr * csg[e] + bsg[e]);
          const float u = rs * acc[2 + t][r] + (nmr * csu[e] + bsu[e]);
          a[4 * hh + e] = g * u * __builtin_amdgcn_rcpf(1.f + __builtin_amdgcn_exp2f(-1.4426950408889634f * g));
        }
      }
      u32x4 o = {pk2(a[0], a[1]), pk2(a[2], a[3]), pk2(a[4], a[5]), pk2(a[6], a[7])};
      *(u32x4*)(ob + t * 4096 + s * 512) = o;
    }
}

extern "C" void kernel_launch(void* const* d_in, const int* in_sizes, int n_in, void* d_out, int out_size, void* d_ws, size_t ws_size, hipStream_t stream) {
  const u16* x = (const u16*)d_in[0]; const u16* kc = (const u16*)d_in[1]; const u16* vc = (const u16*)d_in[2];
  const u16* wq = (const u16*)d_in[3]; const u16* wo = (const u16*)d_in[4];
  const u16* wg = (const u16*)d_in[6]; const u16* wu = (const u16*)d_in[7]; const u16* wd = (const u16*)d_in[8];
  const u16* c = (const u16*)d_in[9]; const u16* wmod = (const u16*)d_in[10];
  char* ws = (char*)d_ws; u16* out = (u16*)d_out;
  constexpr int LDS2 = NSLOT * SLOT, LDS4 = NSLOT * SLOT + 1024, LDS3 = 4 * 64 * RLD * 4 + 2048;
  (void)hipFuncSetAttribute((const void*)k2_attention, hipFuncAttributeMaxDynamicSharedMemorySize, LDS2);
  (void)hipFuncSetAttribute((const void*)k4_gateup, hipFuncAttributeMaxDynamicSharedMemorySize, LDS4);
  (void)hipFuncSetAttribute((const void*)k35_gemm<128, false>, hipFuncAttributeMaxDynamicSharedMemorySize, LDS3);
  (void)hipFuncSetAttribute((const void*)k35_gemm<54, true>, hipFuncAttributeMaxDynamicSharedMemorySize, LDS3);
  hipLaunchKernelGGL(k0_mod_tile, dim3(K0_MODB + K0_TILEB), dim3(256), 0, stream, c, wmod, wo, wg, wu, wd, ws);
  hipLaunchKernelGGL(k1_ln, dim3(64 + 54), dim3(256), 0, stream, x, wg, wu, ws);
  hipLaunchKernelGGL(k2_attention, dim3(256), dim3(512), LDS2, stream, wq, kc, vc, ws);
  hipLaunchKernelGGL((k35_gemm<128, false>), dim3(256), dim3(256), LDS3, stream, (const char*)(ws + WS_O), (const char*)(ws + WS_WOT), x, ws, out);
  hipLaunchKernelGGL(k4_gateup, dim3(216), dim3(512), LDS4, stream, ws);
  hipLaunchKernelGGL((k35_gemm<54, true>), dim3(256), dim3(256), LDS3, stream, (const char*)(ws + WS_ACT), (const char*)(ws + WS_WDT), x, ws, out);
}
```

```cpp
#include <hip/hip_runtime.h>
#include <hip/hip_bf16.h>
#include <stdint.h>
#include <utility>

typedef __bf16   bf16x8 __attribute__((ext_vector_type(8)));
typedef __bf16   bf16x4 __attribute__((ext_vector_type(4)));
typedef __bf16   bf16x2 __attribute__((ext_vector_type(2)));
typedef short    s16x4  __attribute__((ext_vector_type(4)));
typedef float    f32x16 __attribute__((ext_vector_type(16)));
typedef float    f32x4  __attribute__((ext_vector_type(4)));
typedef float    f32x2  __attribute__((ext_vector_type(2)));
typedef unsigned u32x4  __attribute__((ext_vector_type(4)));
typedef unsigned u32x2  __attribute__((ext_vector_type(2)));
typedef unsigned short u16;
#define LAS __attribute__((address_space(3)))
#define DI __device__ __forceinline__
#define SBAR() __builtin_amdgcn_sched_barrier(0)
#define MFMA32(a, b, c) __builtin_amdgcn_mfma_f32_32x32x16_bf16((a), (b), (c), 0, 0, 0)
#define MFMA16(a, b, c) __builtin_amdgcn_mfma_f32_16x16x32_bf16((a), (b), (c), 0, 0, 0)

#define STAMP_DECL
#define STAMP(i)
#define STAMP_FLUSH(k, wv)

constexpr int S = 2048, D = 640, NH = 32, DH = 128, SK = 2048, W = 512, F = 1728, DQ = NH * DH;
constexpr float EPS = 1e-5f;

constexpr size_t WS_MOD  = 0;
constexpr size_t WS_CSBS = 32768;
constexpr size_t WS_STAT = 196608;
constexpr size_t WS_H    = 393216;
constexpr size_t WS_X1   = WS_H + (size_t)S * D * 2;
constexpr size_t WS_Y    = WS_X1 + (size_t)S * D * 2;
constexpr size_t WS_O    = WS_Y + (size_t)S * D * 2;
constexpr size_t WS_ACT  = WS_O + (size_t)S * DQ * 2;
constexpr size_t WS_WOT  = WS_ACT + (size_t)S * F * 2;
constexpr size_t WS_WDT  = WS_WOT + (size_t)DQ * D * 2;
constexpr size_t WS_PART = WS_WDT + (size_t)F * D * 2;
constexpr size_t WS_END  = WS_PART + (size_t)64 * 4 * 3 * 10240;
constexpr size_t WS_CNT  = 163840;
static_assert(WS_CSBS + 27 * 4096 <= WS_CNT && WS_CNT + 16384 <= WS_STAT && WS_STAT + (size_t)S * 64 <= WS_H, "ws map");

DI float bf2f(u16 v) { return __uint_as_float((unsigned)v << 16); }
DI float bflo(unsigned v) { return __uint_as_float(v << 16); }
DI float bfhi(unsigned v) { return __uint_as_float(v & 0xffff0000u); }
DI unsigned pk2(float lo, float hi) { f32x2 v = {lo, hi}; bf16x2 b = __builtin_convertvector(v, bf16x2); return __builtin_bit_cast(unsigned, b); }
DI float rbf(float x) { return bflo(pk2(x, 0.f)); }
DI unsigned rfl(unsigned x) { return __builtin_amdgcn_readfirstlane(x); }
DI int swap23(int i) { return (i & ~12) | ((i & 4) << 1) | ((i & 8) >> 1); }

DI void dma16(unsigned m0v, unsigned voff, __amdgpu_buffer_rsrc_t srd, unsigned soff) {
  asm volatile("s_mov_b32 m0, %0\n\ts_nop 0\n\tbuffer_load_dwordx4 %1, %2, %3 offen lds" :: "s"(m0v), "v"(voff), "s"(srd), "s"(soff) : "memory");
}
template <int OFF> DI void ld16(bf16x8& r, unsigned voff, __amdgpu_buffer_rsrc_t srd, unsigned soff) {
  asm volatile("buffer_load_dwordx4 %0, %1, %2, %3 offen offset:%4" : "=v"(r) : "v"(voff), "s"(srd), "s"(soff), "i"(OFF) : "memory");
}
template <int N> DI void wait_bar4(bf16x8& a, bf16x8& b, bf16x8& c, bf16x8& d) {
  asm volatile("s_waitcnt vmcnt(%4) lgkmcnt(0)\n\ts_barrier" : "+v"(a), "+v"(b), "+v"(c), "+v"(d) : "i"(N) : "memory");
}
template <int N> DI void wait_bar() { asm volatile("s_waitcnt vmcnt(%0) lgkmcnt(0)\n\ts_barrier" :: "i"(N) : "memory"); }
DI bf16x8 lds_rd(const LAS char* p) { return *(const LAS bf16x8*)p; }
DI bf16x8 lds_tr(const LAS char* p0, const LAS char* p1) {
  s16x4 lo = __builtin_amdgcn_ds_read_tr16_b64_v4i16((LAS s16x4*)p0);
  s16x4 hi = __builtin_amdgcn_ds_read_tr16_b64_v4i16((LAS s16x4*)p1);
  return __builtin_bit_cast(bf16x8, __builtin_shufflevector(lo, hi, 0, 1, 2, 3, 4, 5, 6, 7));
}
DI bf16x8 pack8(const f32x16& x, int s, float m) {
  u32x4 w = {pk2(x[8 * s] * m, x[8 * s + 1] * m), pk2(x[8 * s + 2] * m, x[8 * s + 3] * m), pk2(x[8 * s + 4] * m, x[8 * s + 5] * m), pk2(x[8 * s + 6] * m, x[8 * s + 7] * m)};
  return __builtin_bit_cast(bf16x8, w);
}
DI __amdgpu_buffer_rsrc_t mk_srd(const void* p, unsigned bytes) { return __builtin_amdgcn_make_buffer_rsrc((void*)p, (short)0, (int)bytes, 0x00020000); }

constexpr int K1_MOD = 120, K1_LN = 128;
__global__ __launch_bounds__(256) void k1_front(const u16* __restrict__ x, const u16* __restrict__ c, const u16* __restrict__ wmod, char* __restrict__ ws) {
  __shared__ float red[4][64];
  const int tid = threadIdx.x, bid = blockIdx.x, lane = tid & 63, wv = tid >> 6;
  STAMP_DECL; STAMP(0);
  if (bid < K1_MOD) {
    const int sl = bid >> 1, kh = bid & 1, ch = tid & 7, rg = tid >> 3;
    u32x4 wv4[10]; u16 cv[10];
#pragma unroll
    for (int i = 0; i < 10; ++i) wv4[i] = *(const u32x4*)(wmod + (size_t)(kh * 320 + rg + 32 * i) * (6 * D) + sl * 64 + ch * 8);
#pragma unroll
    for (int i = 0; i < 10; ++i) cv[i] = c[kh * 320 + rg + 32 * i];
    float acc[8] = {0, 0, 0, 0, 0, 0, 0, 0};
#pragma unroll
    for (int i = 0; i < 10; ++i) {
      const float a = bf2f(cv[i]), sv = rbf(a * __builtin_amdgcn_rcpf(1.f + __expf(-a)));
#pragma unroll
      for (int e = 0; e < 4; ++e) { acc[2 * e] += sv * bflo(wv4[i][e]); acc[2 * e + 1] += sv * bfhi(wv4[i][e]); }
    }
#pragma unroll
    for (int e = 0; e < 8; ++e) { acc[e] += __shfl_xor(acc[e], 8); acc[e] += __shfl_xor(acc[e], 16); acc[e] += __shfl_xor(acc[e], 32); }
    if (lane < 8) { *(f32x4*)&red[wv][ch * 8] = (f32x4){acc[0], acc[1], acc[2], acc[3]}; *(f32x4*)&red[wv][ch * 8 + 4] = (f32x4){acc[4], acc[5], acc[6], acc[7]}; }
    __syncthreads();
    if (tid < 64) ((float*)(ws + WS_MOD))[kh * 6 * D + sl * 64 + tid] = (red[0][tid] + red[1][tid]) + (red[2][tid] + red[3][tid]);
    STAMP(2); STAMP_FLUSH(1, 0);
    return;
  }
  if (tid < 2) *(unsigned*)(ws + WS_CNT + ((bid - K1_MOD) * 2 + tid) * 64) = 0u;
  const int row = (bid - K1_MOD) * 16 + wv * 4 + (lane >> 4), cg = lane & 15;
  u32x4 xv[5];
#pragma unroll
  for (int i = 0; i < 5; ++i) xv[i] = *(const u32x4*)(x + (size_t)row * D + (cg + 16 * i) * 8);
  float s = 0.f;
#pragma unroll
  for (int i = 0; i < 5; ++i)
#pragma unroll
    for (int e = 0; e < 4; ++e) s += bflo(xv[i][e]) + bfhi(xv[i][e]);
  s += __shfl_xor(s, 1); s += __shfl_xor(s, 2); s += __shfl_xor(s, 4); s += __shfl_xor(s, 8);
  const float m = s * (1.f / D);
  float q = 0.f;
#pragma unroll
  for (int i = 0; i < 5; ++i)
#pragma unroll
    for (int e = 0; e < 4; ++e) { const float a = bflo(xv[i][e]) - m, b = bfhi(xv[i][e]) - m; q += a * a + b * b; }
  q += __shfl_xor(q, 1); q += __shfl_xor(q, 2); q += __shfl_xor(q, 4); q += __shfl_xor(q, 8);
  const float r = rsqrtf(q * (1.f / D) + EPS);
#pragma unroll
  for (int i = 0; i < 5; ++i) {
    const int chn = cg + 16 * i;
    u32x4 o;
#pragma unroll
    for (int e = 0; e < 4; ++e) {
      const unsigned lo = pk2((bflo(xv[i][e]) - m) * r, 0.f) & 0xffffu, hb = __float_as_uint((bfhi(xv[i][e]) - m) * r);
      const unsigned mag = (hb & 0x7fffffffu) + 0x8000u;
      o[e] = (hb & 0x80000000u) | ((mag >= lo ? mag - lo : 0u) & 0xffff0000u) | lo;
    }
    *(u32x4*)(ws + WS_H + ((size_t)(row >> 5) * 40 + (chn >> 1)) * 1024 + ((row & 31) + 32 * (chn & 1)) * 16) = o;
  }
  STAMP(1); STAMP_FLUSH(1, 0);
}

constexpr int SLOT = 16384, NSLOT = 8;
constexpr float QSCALE = 0.08838834764831845f * 1.4426950408889634f;
constexpr float RTHR = 64.f;

struct K2C {
  __amdgpu_buffer_rsrc_t srdW, srdK, srdV, srdH;
  unsigned voffW, voffK, voffV, voffH, m0base, sofW, sofKV, sofH;
  unsigned aW, aK0, aK1, aV;
  const LAS char *pW, *pK0, *pK1, *pV;
  bf16x8 f[4];
  bf16x8 qf[8];
  f32x16 o[4], sA, sB, negm;
  u32x4 pA[2], pB[2];
  float l0, l1, fpend, aone; int pend;
  __amdgpu_buffer_rsrc_t srdSs, srdSd;
  __amdgpu_buffer_rsrc_t srdG, srdU, srdT; unsigned sofT, sofT2;
  unsigned voffS, sofSs, sofSd[4], m0S; const LAS char* pS; bf16x8 sf[2];
  float ca[20], cb[20], mt[2][3];
};
#define PIN(x) asm volatile("" : "+v"(x))
DI const LAS char* slot_ptr(unsigned lanebase, int tile) { unsigned a = lanebase + (tile & (NSLOT - 1)) * SLOT; PIN(a); return (const LAS char*)(size_t)a; }
template <int I, int HALF> DI void k2_dma1(const K2C& c) {
  const unsigned m0v = c.m0base + (I & (NSLOT - 1)) * SLOT + HALF * 1024;
  if constexpr (I < 10) dma16(m0v, c.voffW, c.srdW, c.sofW + I * 64 * 8192 + HALF * 128);
  else if constexpr (((I - 10) & 1) == 0) dma16(m0v, c.voffK, c.srdK, c.sofKV + ((I - 10) >> 1) * 16384 + HALF * 128);
  else dma16(m0v, c.voffV, c.srdV, c.sofKV + ((I - 10) >> 1) * 16384 + HALF * 128);
}
template <int I> DI void k2_dma(const K2C& c) { k2_dma1<I, 0>(c); k2_dma1<I, 1>(c); }
template <int I, int S_> DI void k2_ldh1(const K2C& c, bf16x8& r) { ld16<S_ * 1024>(r, c.voffH, c.srdH, c.sofH + I * 4096); }
template <int I> DI void k2_ldh(const K2C& c, bf16x8 (&hf)[4]) { k2_ldh1<I, 0>(c, hf[0]); k2_ldh1<I, 1>(c, hf[1]); k2_ldh1<I, 2>(c, hf[2]); k2_ldh1<I, 3>(c, hf[3]); }
template <int N> DI void wait_bar4v(bf16x8& a, bf16x8& b, bf16x8& c, bf16x8& d) {
  asm volatile("s_waitcnt vmcnt(%4)\n\ts_barrier" : "+v"(a), "+v"(b), "+v"(c), "+v"(d) : "i"(N) : "memory");
}
template <int N> DI void wait_barv() { asm volatile("s_waitcnt vmcnt(%0)\n\ts_barrier" :: "i"(N) : "memory"); }
DI bf16x8 rdW(const K2C& c, int g) { const LAS char* p = c.pW + (g >> 2) * 4096 + (g & 3) * 512; return lds_tr(p, p + 256); }
DI bf16x8 rdK(const K2C& c, int kb, int s) { return lds_rd(((s & 1) ? c.pK1 : c.pK0) + kb * 8192 + (s >> 1) * 512); }
DI bf16x8 rdV(const K2C& c, int kb, int g) { const LAS char* p = c.pV + (2 * kb + (g >> 2)) * 4096 + (g & 3) * 512; return lds_tr(p, p + 2048); }

template <int N> DI float bcast(float v) { return __int_as_float(__builtin_amdgcn_update_dpp(0, __float_as_int(v), 0x150 + N, 0xf, 0xf, true)); }
template <int U> DI void mod_a(K2C& c, bf16x8 (&hf)[10][4]) {
  if constexpr (U < 160) {
    const u32x4 v = __builtin_bit_cast(u32x4, hf[U >> 4][(U >> 2) & 3]);
    c.mt[U & 1][0] = bflo(v[U & 3]); c.mt[U & 1][1] = bcast<(2 * U) & 15>(c.cb[U >> 3]); c.mt[U & 1][2] = bcast<((2 * U) & 15) + 1>(c.cb[U >> 3]);
    PIN(c.mt[U & 1][0]); PIN(c.mt[U & 1][1]); PIN(c.mt[U & 1][2]);
  }
}
template <int U> DI void mod_b(K2C& c, bf16x8 (&hf)[10][4]) {
  if constexpr (U < 160) {
    u32x4 v = __builtin_bit_cast(u32x4, hf[U >> 4][(U >> 2) & 3]);
    float lo = c.mt[U & 1][1], hi = c.mt[U & 1][2];
    asm("v_fmac_f32_dpp %0, %1, %2 row_newbcast:%3 row_mask:0xf bank_mask:0xf" : "+v"(lo) : "v"(c.ca[U >> 3]), "v"(c.mt[U & 1][0]), "i"((2 * U) & 15));
    asm("v_fmac_f32_dpp %0, %1, %2 row_newbcast:%3 row_mask:0xf bank_mask:0xf" : "+v"(hi) : "v"(c.ca[U >> 3]), "v"(v[U & 3]), "i"(((2 * U) & 15) + 1));
    v[U & 3] = pk2(lo, hi);
    hf[U >> 4][(U >> 2) & 3] = __builtin_bit_cast(bf16x8, v); PIN(hf[U >> 4][(U >> 2) & 3]);
  }
}
template <int I, int G> DI void k2_qgap(K2C& c, bf16x8 (&hf)[10][4]) {
  c.o[G & 3] = MFMA32(c.f[G & 3], hf[I][G >> 2], c.o[G & 3]);
  if constexpr (G < 12) c.f[G & 3] = rdW(c, G + 4);
  else if constexpr (I < 9) c.f[G & 3] = rdW(c, G - 12);
  else c.f[G & 3] = rdK(c, 0, G - 12);
  if constexpr (G == 11) { if constexpr (I < 9) c.pW = slot_ptr(c.aW, I + 1); else { c.pK0 = slot_ptr(c.aK0, 10); c.pK1 = slot_ptr(c.aK1, 10); } }
  if constexpr (I == 0) {
    if constexpr (G < 4) k2_ldh1<2, G>(c, hf[2][G]);
    if constexpr (G == 4 || G == 5) k2_dma1<2, (G == 5)>(c);
    if constexpr (G >= 9 && G < 13) k2_ldh1<3, G - 9>(c, hf[3][G - 9]);
    if constexpr (G == 13 || G == 14) k2_dma1<3, (G == 14)>(c);
  } else if constexpr (G < 8 && (G & 1)) {
    if constexpr (I + 3 < 10) k2_ldh1<I + 3, (G >> 1)>(c, hf[I + 3][G >> 1]);
    else if constexpr (I == 7) k2_dma1<10 + (G >> 2), (G >> 1) & 1>(c);
    else if constexpr (I == 8) k2_dma1<13 + (G >> 2), (G >> 1) & 1>(c);
  }
  mod_b<16 * I + G + 6>(c, hf);
  mod_a<16 * I + G + 7>(c, hf);
  if constexpr (G == 8) { if constexpr (I < 9) wait_bar4v<(I == 0 ? 6 : 10)>(hf[I + 1][0], hf[I + 1][1], hf[I + 1][2], hf[I + 1][3]); else wait_barv<10>(); }
  if constexpr (I > 0 && (G == 10 || G == 13)) k2_dma1<(I < 7 ? I + 3 : I == 7 ? 12 : I == 8 ? 15 : 16), (G == 13)>(c);
  SBAR();
}
template <int I> DI void k2_qproj(K2C& c, bf16x8 (&hf)[10][4]) {
  k2_qgap<I, 0>(c, hf); k2_qgap<I, 1>(c, hf); k2_qgap<I, 2>(c, hf); k2_qgap<I, 3>(c, hf); k2_qgap<I, 4>(c, hf); k2_qgap<I, 5>(c, hf); k2_qgap<I, 6>(c, hf); k2_qgap<I, 7>(c, hf);
  k2_qgap<I, 8>(c, hf); k2_qgap<I, 9>(c, hf); k2_qgap<I, 10>(c, hf); k2_qgap<I, 11>(c, hf); k2_qgap<I, 12>(c, hf); k2_qgap<I, 13>(c, hf); k2_qgap<I, 14>(c, hf); k2_qgap<I, 15>(c, hf);
}
#define MX3(a, b, c) __builtin_fmaxf(__builtin_fmaxf((a), (b)), (c))
#define EX2(x) __builtin_amdgcn_exp2f(x)
DI float max16(const f32x16& s) {
  float a = MX3(s[0], s[1], s[2]), b = MX3(s[3], s[4], s[5]);
  a = MX3(a, s[6], s[7]); b = MX3(b, s[8], s[9]); a = MX3(a, s[10], s[11]); b = MX3(b, s[12], s[13]); a = MX3(a, s[14], s[15]);
  return __builtin_fmaxf(a, b);
}
DI float xhalf_max(float a) { auto rr = __builtin_amdgcn_permlane32_swap(__float_as_uint(a), __float_as_uint(a), false, false); return __builtin_fmaxf(__uint_as_float(rr[0]), __uint_as_float(rr[1])); }
template <int G> DI void sm_second(K2C& c, f32x16& s, u32x4 (&p)[2]) {
  if constexpr (G < 4) { s[8 + 2 * G] = EX2(s[8 + 2 * G]); s[9 + 2 * G] = EX2(s[9 + 2 * G]); p[0][G] = pk2(s[2 * G], s[2 * G + 1]); PIN(p[0]); }
  if constexpr (G < 5) { c.l0 += s[6 + 2 * G]; c.l1 += s[7 + 2 * G]; PIN(c.l0); PIN(c.l1); }
  if constexpr (G == 4) { p[1][0] = pk2(s[8], s[9]); PIN(p[1]); }
  if constexpr (G == 5) { p[1][1] = pk2(s[10], s[11]); p[1][2] = pk2(s[12], s[13]); PIN(p[1]); }
  if constexpr (G == 6) { p[1][3] = pk2(s[14], s[15]); PIN(p[1]); }
  if constexpr (G < 4) PIN(s);
}
DI int imx3(int a, float b, float c) { return max(max(a, __float_as_int(b)), __float_as_int(c)); }
template <int G> DI void sm_first(K2C& c, f32x16& s, int& mx) {
  if constexpr (G == 2) { mx = imx3(__float_as_int(s[0]), s[1], s[2]); mx = imx3(mx, s[3], s[4]); mx = imx3(mx, s[5], s[6]); mx = imx3(mx, s[7], s[8]); PIN(mx); }
  if constexpr (G == 3) {
    mx = imx3(mx, s[9], s[10]); mx = imx3(mx, s[11], s[12]); mx = imx3(mx, s[13], s[14]); mx = max(mx, __float_as_int(s[15]));
    float t0, t1;
    asm volatile("v_cmp_lt_i32 vcc, %[thr], %[mx]\n\ts_mov_b32 %[pend], 0\n\ts_cbranch_vccz 1f\n\t"
                 "v_mov_b32 %[t0], %[mx]\n\tv_mov_b32 %[t1], %[mx]\n\ts_nop 1\n\tv_permlane32_swap_b32 %[t0], %[t1]\n\ts_nop 1\n\t"
                 "v_max_i32 %[t0], %[t0], %[t1]\n\tv_max_i32 %[t0], 0, %[t0]\n\t"
                 "v_exp_f32 %[f], -%[t0]\n\tv_sub_f32 %[t1], 0, %[t0]\n\ts_nop 1\n\t"
                 "v_mul_f32 %[l0], %[l0], %[f]\n\tv_mul_f32 %[l1], %[l1], %[f]\n\t"
                 "v_mfma_f32_32x32x2_f32 %[s], %[one], %[t1], %[s]\n\tv_mfma_f32_32x32x2_f32 %[ng], %[one], %[t1], %[ng]\n\t"
                 "s_mov_b32 %[pend], 1\n\ts_nop 15\n\ts_nop 7\n1:"
                 : [s] "+v"(s), [ng] "+v"(c.negm), [l0] "+v"(c.l0), [l1] "+v"(c.l1), [f] "+v"(c.fpend), [pend] "=&s"(c.pend), [t0] "=&v"(t0), [t1] "=&v"(t1)
                 : [mx] "v"(mx), [thr] "s"(__float_as_int(RTHR)), [one] "v"(c.aone) : "vcc", "memory");
  }
  if constexpr (G >= 4) { s[2 * G - 8] = EX2(s[2 * G - 8]); s[2 * G - 7] = EX2(s[2 * G - 7]); PIN(s); }
  if constexpr (G >= 5) { c.l0 += s[2 * G - 10]; c.l1 += s[2 * G - 9]; PIN(c.l0); PIN(c.l1); }
}
DI void apply_pend(K2C& c) {
  asm volatile("" ::: "memory");
  if (__builtin_expect(c.pend != 0, 0)) {
#pragma unroll
    for (int dt = 0; dt < 4; ++dt) c.o[dt] *= c.fpend;
  }
}
constexpr int BA7 = 9;
constexpr int ba_wait(int t) { return t == 0 ? 8 : t <= 4 ? 10 : t == 5 ? 6 : t == 6 ? 6 : BA7; }
template <int P> DI void side_ld(const K2C& c) { dma16(c.m0S + P * 1024, c.voffS, c.srdSs, c.sofSs + P * 8 * D * 2); }
template <int P> DI void side_rd(K2C& c) { const LAS char* p = c.pS + (P >> 1) * 512 + (P & 1) * 32; c.sf[P & 1] = lds_tr(p, p + 256); }
template <int P> DI void side_st(const K2C& c) { __builtin_amdgcn_raw_buffer_store_b128(__builtin_bit_cast(u32x4, c.sf[P & 1]), c.srdSd, c.voffH, c.sofSd[P], 16); }
template <int T, int G> DI void k2_g1(K2C& c) {
  c.sB = MFMA32(c.f[G & 3], c.qf[G], G == 0 ? c.negm : c.sB);
  if constexpr (G == 2) { wait_barv<ba_wait(T)>(); c.pV = slot_ptr(c.aV, 11 + 2 * T); }
  if constexpr (G < 4) c.f[G & 3] = rdK(c, 1, G + 4); else c.f[G & 3] = rdV(c, 0, G - 4);
  if constexpr (G == 6 || G == 7) { if constexpr (T == 0) k2_dma1<17, (G == 7)>(c); else if constexpr (T <= 4) k2_dma1<16 + 2 * T, (G == 7)>(c); }
  sm_second<G>(c, c.sA, c.pA);
  SBAR();
}
template <int T, int G> DI void k2_g2(K2C& c, int& mx) {
  c.o[G & 3] = MFMA32(c.f[G & 3], __builtin_bit_cast(bf16x8, c.pA[G >> 2]), c.o[G & 3]);
  if constexpr (G == 3 && T < 7) { c.pK0 = slot_ptr(c.aK0, 12 + 2 * T); c.pK1 = slot_ptr(c.aK1, 12 + 2 * T); }
  if constexpr (G < 4) c.f[G & 3] = rdV(c, 0, G + 4); else if constexpr (T < 7) c.f[G & 3] = rdK(c, 0, G - 4); else c.f[G & 3] = rdV(c, 1, G - 4);
  if constexpr ((G == 0 || G == 1) && T >= 1 && T <= 4) k2_dma1<17 + 2 * T, (G == 1)>(c);
  sm_first<G>(c, c.sB, mx);
  SBAR();
}
template <int T, int G> DI void k2_g3(K2C& c) {
  if constexpr (T < 7) {
    c.sA = MFMA32(c.f[G & 3], c.qf[G], G == 0 ? c.negm : c.sA);
    if constexpr (G < 4) c.f[G & 3] = rdK(c, 0, G + 4); else c.f[G & 3] = rdV(c, 1, G - 4);
  }
  if constexpr (T == 0 && G >= 5) side_ld<G - 5>(c);
  if constexpr (T == 2 && G == 5) { asm volatile("s_waitcnt vmcnt(8)" ::: "memory"); side_rd<0>(c); }
  if constexpr (T == 2 && G == 6) side_rd<1>(c);
  if constexpr (T == 5 && G >= 4) dma16(c.m0S, c.voffH, (G & 1) ? c.srdU : c.srdG, c.sofT + (G >= 6 ? 2048 * 1024 : 0));
  if constexpr (T == 6 && G >= 3) dma16(c.m0S, c.voffH, c.srdT, c.sofT2 + (G - 3) * 1024);
  sm_second<G>(c, c.sB, c.pB);
  SBAR();
}
template <int T, int G> DI void k2_g4(K2C& c, int& mx) {
  c.o[G & 3] = MFMA32(c.f[G & 3], __builtin_bit_cast(bf16x8, c.pB[G >> 2]), c.o[G & 3]);
  if constexpr (G < 4) c.f[G & 3] = rdV(c, 1, G + 4); else if constexpr (T < 7) c.f[G & 3] = rdK(c, 1, G - 4);
  if constexpr (T == 0 && G == 0) side_ld<3>(c);
  if constexpr (T == 2 && G == 0) { side_st<0>(c); side_rd<2>(c); }
  if constexpr (T == 2 && G == 1) { side_st<1>(c); side_rd<3>(c); }
  if constexpr (T == 2 && G == 5) side_st<2>(c);
  if constexpr (T == 2 && G == 6) side_st<3>(c);
  if constexpr (T < 7) sm_first<G>(c, c.sA, mx);
  SBAR();
}
template <int T> DI void k2_period(K2C& c) {
  int mx;
  k2_g1<T, 0>(c); k2_g1<T, 1>(c); k2_g1<T, 2>(c); k2_g1<T, 3>(c); k2_g1<T, 4>(c); k2_g1<T, 5>(c); k2_g1<T, 6>(c); k2_g1<T, 7>(c);
  k2_g2<T, 0>(c, mx); k2_g2<T, 1>(c, mx); k2_g2<T, 2>(c, mx); k2_g2<T, 3>(c, mx); k2_g2<T, 4>(c, mx); k2_g2<T, 5>(c, mx); k2_g2<T, 6>(c, mx); k2_g2<T, 7>(c, mx);
  apply_pend(c);
  k2_g3<T, 0>(c); k2_g3<T, 1>(c); k2_g3<T, 2>(c); k2_g3<T, 3>(c); k2_g3<T, 4>(c); k2_g3<T, 5>(c); k2_g3<T, 6>(c); k2_g3<T, 7>(c);
  k2_g4<T, 0>(c, mx); k2_g4<T, 1>(c, mx); k2_g4<T, 2>(c, mx); k2_g4<T, 3>(c, mx); k2_g4<T, 4>(c, mx); k2_g4<T, 5>(c, mx); k2_g4<T, 6>(c, mx); k2_g4<T, 7>(c, mx);
  if constexpr (T < 7) apply_pend(c);
}

__global__ __launch_bounds__(512, 2) void k2_attention(const u16* __restrict__ wq, const u16* __restrict__ kc, const u16* __restrict__ vc,
                                                        const u16* __restrict__ wo, const u16* __restrict__ wd, const u16* __restrict__ wg, const u16* __restrict__ wu, char* __restrict__ ws) {
  extern __shared__ __attribute__((aligned(16))) char smem[];
  const int tid = threadIdx.x, lane = tid & 63, bid = blockIdx.x;
  const unsigned w = rfl(tid >> 6);
  const int head = ((bid & 7) >> 1) * 8 + ((bid >> 3) & 7), qb = (bid & 1) * 4 + (bid >> 6);
  const int r32 = lane & 31, h = lane >> 5;
  STAMP_DECL; STAMP(0);
  K2C c;
  c.srdW = mk_srd(wq, D * DQ * 2); c.srdK = mk_srd(kc, NH * SK * DH * 2); c.srdV = mk_srd(vc, NH * SK * DH * 2); c.srdH = mk_srd(ws + WS_H, S * D * 2);
  {
    const unsigned row = (lane >> 2) & 7, sub = lane >> 5, ch = lane & 3;
    c.voffW = row * 8192 + sub * 64 + ch * 16;
    c.voffV = row * 256 + sub * 64 + ch * 16;
    c.voffK = row * 256 + sub * 64 + ((ch ^ ((2 * w + (row >> 2)) & 3)) * 16);
    c.voffH = lane * 16;
    const unsigned sb = (unsigned)(size_t)(LAS char*)smem;
    c.m0base = sb + w * 2048;
    c.sofW = w * 8 * 8192 + head * 256;
    c.sofKV = (head * SK + (SK - W) + w * 8) * 256;
    c.sofH = (qb * 8 + w) * 40 * 1024;
    const int blk = (lane >> 4) & 1, q = (lane >> 2) & 3, p = lane & 3, sp = ((p & 1) << 1) | (p >> 1);
    c.aW = sb + h * 2048 + q * 64 + blk * 32 + sp * 8;
    c.aV = sb + (4 * h + q) * 64 + blk * 32 + sp * 8;
    c.aK0 = sb + 2048 * (r32 >> 3) + 64 * (r32 & 7) + 16 * (h ^ ((r32 >> 2) & 3));
    c.aK1 = c.aK0 ^ 32;
    const unsigned u = bid * 8 + w; const bool isO = u < 1280, live = u < 1820;
    const unsigned uu = isO ? u : u - 1280, ks = uu / 10, nb = uu - ks * 10, KSn = isO ? 128 : 54;
    c.srdSs = mk_srd(isO ? wo : wd, live ? (isO ? DQ * D * 2 : F * D * 2) : 0);
    c.srdSd = mk_srd(ws + (isO ? WS_WOT : WS_WDT), live ? (isO ? DQ * D * 2 : F * D * 2) : 0);
    c.voffS = row * (D * 2) + sub * 64 + ch * 16;
    c.sofSs = ks * 32 * (D * 2) + nb * 128;
#pragma unroll
    for (int i = 0; i < 4; ++i) { const unsigned C = 4 * nb + i, nt = C / 5; c.sofSd[i] = ((nt * KSn + ks) * 5 + (C - nt * 5)) * 1024; }
    c.srdG = mk_srd(wg, D * F * 2); c.srdU = mk_srd(wu, D * F * 2); c.sofT = u * 1024;
    c.srdT = mk_srd(ws + WS_WOT, DQ * D * 2); c.sofT2 = ((head & 7) * 128 + (head >> 3) * 32) * 5120 + ((bid >> 6) * 40 + w * 5) * 1024;
    c.m0S = sb + NSLOT * SLOT + w * 4096;
    c.pS = (const LAS char*)(size_t)(c.m0S + (lane >> 4) * 1024 + q * 64 + p * 8);
  }
  asm volatile("s_nop 4" ::: "memory");
  bf16x8 hf[10][4];
  u32x2 m0_, m1_, m2_, m3_;
  {
    const __amdgpu_buffer_rsrc_t srdM = mk_srd(ws + WS_MOD, 2 * 6 * D * 4); const unsigned vo = min(tid, 319) * 8;
    asm volatile("buffer_load_dwordx2 %0, %4, %5, 0 offen\n\tbuffer_load_dwordx2 %1, %4, %5, 0 offen offset:2560\n\t"
                 "buffer_load_dwordx2 %2, %4, %5, %6 offen\n\tbuffer_load_dwordx2 %3, %4, %5, %6 offen offset:2560"
                 : "=&v"(m0_), "=&v"(m1_), "=&v"(m2_), "=&v"(m3_) : "v"(vo), "s"(srdM), "s"(6 * D * 4) : "memory");
  }
  k2_ldh<0>(c, hf[0]); k2_dma<0>(c);
  asm volatile("s_barrier" ::: "memory");
  k2_ldh<1>(c, hf[1]); k2_dma<1>(c);
  asm volatile("s_waitcnt vmcnt(12)" : "+v"(m0_), "+v"(m1_), "+v"(m2_), "+v"(m3_) :: "memory");
  {
    const f32x2 s0 = __builtin_bit_cast(f32x2, m0_) + __builtin_bit_cast(f32x2, m2_), c0 = __builtin_bit_cast(f32x2, m1_) + __builtin_bit_cast(f32x2, m3_);
    const int t = min(tid, 319), e = 8 * (t >> 3) + 2 * (t & 3), hh = (t >> 2) & 1;
    LAS float* d = (LAS float*)((LAS char*)smem + NSLOT * SLOT) + (hh * 16 + (e & 15)) * 20 + (e >> 4);
    d[0] = rbf(1.f + rbf(c0[0])); d[20] = rbf(1.f + rbf(c0[1])); d[640] = rbf(s0[0]); d[660] = rbf(s0[1]);
  }
  asm volatile("s_waitcnt lgkmcnt(0)\n\ts_barrier" ::: "memory");
  {
    const LAS f32x4* d = (const LAS f32x4*)((LAS char*)smem + NSLOT * SLOT + (h * 16 + (lane & 15)) * 80);
#pragma unroll
    for (int i = 0; i < 5; ++i) { const f32x4 va = d[i], vb = d[160 + i];
#pragma unroll
      for (int e = 0; e < 4; ++e) { c.ca[4 * i + e] = va[e]; c.cb[4 * i + e] = vb[e]; } }
  }
#pragma unroll
  for (int dt = 0; dt < 4; ++dt) c.o[dt] = f32x16{};
  c.pW = slot_ptr(c.aW, 0);
  STAMP(1);
  wait_bar4v<6>(hf[0][0], hf[0][1], hf[0][2], hf[0][3]);
  STAMP(2);
#pragma unroll
  for (int g = 0; g < 4; ++g) c.f[g] = rdW(c, g);
  mod_a<0>(c, hf); mod_a<1>(c, hf); SBAR(); mod_b<0>(c, hf); mod_a<2>(c, hf); SBAR(); mod_b<1>(c, hf); mod_a<3>(c, hf); SBAR(); mod_b<2>(c, hf); mod_a<4>(c, hf); SBAR();
  mod_b<3>(c, hf); mod_a<5>(c, hf); SBAR(); mod_b<4>(c, hf); mod_a<6>(c, hf); SBAR(); asm volatile("s_nop 1"); mod_b<5>(c, hf);
  SBAR();
  k2_qproj<0>(c, hf); k2_qproj<1>(c, hf); k2_qproj<2>(c, hf); k2_qproj<3>(c, hf); k2_qproj<4>(c, hf);
  STAMP(3);
  k2_qproj<5>(c, hf); k2_qproj<6>(c, hf); k2_qproj<7>(c, hf); k2_qproj<8>(c, hf); k2_qproj<9>(c, hf);
  STAMP(4);
#pragma unroll
  for (int s = 0; s < 8; ++s) c.qf[s] = pack8(c.o[s >> 1], s & 1, QSCALE);
#pragma unroll
  for (int dt = 0; dt < 4; ++dt) c.o[dt] = f32x16{};
  SBAR();
  c.sA = f32x16{};
#pragma unroll
  for (int g = 0; g < 8; ++g) { c.sA = MFMA32(c.f[g & 3], c.qf[g], c.sA); c.f[g & 3] = g < 4 ? rdK(c, 0, g + 4) : rdK(c, 1, g - 4); SBAR(); }
  const float mref = xhalf_max(max16(c.sA)); c.fpend = 1.f; c.aone = h ? 0.f : 1.f;
#pragma unroll
  for (int r = 0; r < 16; ++r) { c.sA[r] -= mref; c.negm[r] = -mref; }
  PIN(c.negm);
#pragma unroll
  for (int r = 0; r < 8; ++r) c.sA[r] = EX2(c.sA[r]);
  c.l0 = (c.sA[0] + c.sA[2]) + c.sA[4]; c.l1 = (c.sA[1] + c.sA[3]) + c.sA[5];
  SBAR();
  STAMP(5);
  k2_period<0>(c); STAMP(6); k2_period<1>(c); k2_period<2>(c); k2_period<3>(c); STAMP(7);
  k2_period<4>(c); k2_period<5>(c); k2_period<6>(c); k2_period<7>(c);
  STAMP(8);
  float lsum = c.l0 + c.l1;
  { auto rr = __builtin_amdgcn_permlane32_swap(__float_as_uint(lsum), __float_as_uint(lsum), false, false); lsum = __uint_as_float(rr[0]) + __uint_as_float(rr[1]); }
  const float il = 1.f / lsum;
  const int qg = qb * 256 + w * 32 + r32;
  char* ob = ws + WS_O + ((size_t)((qg >> 5) * 128 + head * 4) * 2 + ((qg >> 4) & 1)) * 1024 + ((qg & 15) + 16 * h) * 16;
#pragma unroll
  for (int dt = 0; dt < 4; ++dt)
#pragma unroll
    for (int s = 0; s < 2; ++s) *(bf16x8*)(ob + dt * 2048 + s * 512) = pack8(c.o[dt], s, il);
  STAMP(9); STAMP_FLUSH(2, STAMP_WAVE);
}

typedef __attribute__((address_space(1))) unsigned gu32;
constexpr int RLD = 84;
constexpr int K3_OWN = 131072, K3_MODL = K3_OWN + 64 * RLD * 4, K3_CSL = K3_MODL + 2048, LDS3 = K3_CSL + 4096;
struct K3C {
  __amdgpu_buffer_rsrc_t srdW, srdO, srdM, srdS, srdX; unsigned voff, m0base, sofW, sofO, voffM, voffS[3], voffX;
  const LAS char *pLo, *pHi; LAS char* sm; bf16x8 fw[5], sw[3], xv[2]; f32x4 acc[2][5]; float m0_, m1_; float* csdst; int tid; bool plus1;
};
constexpr int k3_wr(int j) { return j < 6 ? 2 : (j >= 11 && j < 15) ? 1 : 0; }
constexpr int k3_ex(int j) { return j == 28 ? 5 : j == 29 ? 2 : 0; }
constexpr int k3_ops(int j) { return k3_wr(j) + k3_ex(j) + (j + 4 < 32 ? 2 : 0); }
constexpr int k3_wait(int j) { return j == 0 ? 8 : j == 1 ? 6 + k3_ops(0) : j == 2 ? 2 + k3_ops(0) + k3_ops(1) : k3_ops(j - 3) + k3_ops(j - 2) + k3_ops(j - 1); }
template <int R> DI void k3_dw(const K3C& c) { dma16(c.m0base + (R & 15) * 8192, c.voff, c.srdW, c.sofW + R * 8192); }
template <int J, int R2> DI void k3_lo(const K3C& c, bf16x8& r) { ld16<R2 * 1024>(r, c.voff, c.srdO, c.sofO + J * 2048); }
DI bf16x8 k3_rd(const K3C& c, int piece) { const int pp = piece & 127; return lds_rd(pp < 64 ? c.pLo + pp * 1024 : c.pHi + (pp - 64) * 1024); }
DI float xor8(float v) { return __int_as_float(__builtin_amdgcn_update_dpp(0, __float_as_int(v), 0x128, 0xf, 0xf, true)); }
DI float sum816(float v) {
  v += xor8(v);
  { auto r = __builtin_amdgcn_permlane16_swap(__float_as_uint(v), __float_as_uint(v), false, false); v = __uint_as_float(r[0]) + __uint_as_float(r[1]); }
  { auto r = __builtin_amdgcn_permlane32_swap(__float_as_uint(v), __float_as_uint(v), false, false); v = __uint_as_float(r[0]) + __uint_as_float(r[1]); }
  return v;
}
template <int J> DI void k3_step(K3C& c, bf16x8 (&of)[32][2]) {
  if constexpr (J > 0) {
    if constexpr (J == 2 || J == 5 || J == 11 || J == 20) asm volatile("s_waitcnt vmcnt(%2)\n\ts_barrier" : "+v"(of[J][0]), "+v"(of[J][1]) : "i"(k3_wait(J)) : "memory");
    else asm volatile("s_waitcnt vmcnt(%2)" : "+v"(of[J][0]), "+v"(of[J][1]) : "i"(k3_wait(J)) : "memory");
  }
#pragma unroll
  for (int cc = 0; cc < 5; ++cc) {
    c.acc[0][cc] = MFMA16(c.fw[cc], of[J][0], c.acc[0][cc]);
    c.acc[1][cc] = MFMA16(c.fw[cc], of[J][1], c.acc[1][cc]);
    if (J < 31) c.fw[cc] = k3_rd(c, 5 * (J + 1) + cc);
    if constexpr (k3_wr(J) == 2) { if (cc == 0) k3_dw<4 + 2 * J>(c); if (cc == 1) k3_dw<5 + 2 * J>(c); }
    if constexpr (k3_wr(J) == 1) { if (cc == 0) k3_dw<5 + J>(c); }
    if constexpr (J == 28) { if (cc == 0) asm volatile("buffer_load_dword %0, %2, %3, 0 offen\n\tbuffer_load_dword %1, %2, %3, %4 offen" : "=&v"(c.m0_), "=&v"(c.m1_) : "v"(c.voffM), "s"(c.srdM), "s"(6 * D * 4) : "memory"); }
    if constexpr (J == 28) { if (cc >= 1 && cc < 4) ld16<0>(c.sw[cc - 1], c.voffS[cc - 1], c.srdS, 0); }
    if constexpr (J == 29) { if (cc == 0) ld16<0>(c.xv[0], c.voffX, c.srdX, 0); if (cc == 1) ld16<128>(c.xv[1], c.voffX, c.srdX, 0); }
    if constexpr (J + 4 < 32) { if (cc == 2) k3_lo<J + 4, 0>(c, of[J + 4][0]); if (cc == 3) k3_lo<J + 4, 1>(c, of[J + 4][1]); }
    SBAR();
  }
}
template <int... Js> DI void k3_steps(K3C& c, bf16x8 (&of)[32][2], std::integer_sequence<int, Js...>) { (k3_step<Js>(c, of), ...); }
__global__ __launch_bounds__(512, 2) void k3_oproj(const u16* __restrict__ x, const u16* __restrict__ wg, const u16* __restrict__ wu, char* __restrict__ ws) {
  extern __shared__ __attribute__((aligned(16))) char smem[];
  LAS char* sm = (LAS char*)smem;
  const int tid = threadIdx.x, lane = tid & 63, bid = blockIdx.x;
  const unsigned w = rfl(tid >> 6);
  const int kq = (bid & 7) >> 1, rb = (bid & 1) * 4 + (bid >> 6), nt = (bid >> 3) & 7;
  STAMP_DECL; STAMP(0);
  K3C c;
  c.srdW = mk_srd(ws + WS_WOT, DQ * D * 2); c.srdO = mk_srd(ws + WS_O, S * DQ * 2);
  c.voff = lane * 16; c.m0base = (unsigned)(size_t)sm + w * 1024;
  c.sofW = (nt * 128 + kq * 32) * 5120 + w * 1024; c.sofO = ((rb * 8 + w) * 128 + kq * 32) * 2048;
  asm volatile("s_nop 4" ::: "memory");
  bf16x8 of[32][2];
  k3_dw<0>(c); k3_dw<1>(c); k3_lo<0, 0>(c, of[0][0]); k3_lo<0, 1>(c, of[0][1]);
  asm volatile("s_barrier" ::: "memory");
  k3_lo<1, 0>(c, of[1][0]); k3_lo<1, 1>(c, of[1][1]); k3_dw<2>(c); k3_dw<3>(c);
  asm volatile("s_barrier" ::: "memory");
  k3_lo<2, 0>(c, of[2][0]); k3_lo<2, 1>(c, of[2][1]); k3_lo<3, 0>(c, of[3][0]); k3_lo<3, 1>(c, of[3][1]);
  c.pLo = sm + lane * 16; { unsigned a = (unsigned)(size_t)sm + lane * 16 + 65536; PIN(a); c.pHi = (const LAS char*)(size_t)a; }
  c.sm = sm; c.tid = tid;
  const int frow = rb * 256 + kq * 64 + w * 8 + (lane & 7), fch = lane >> 3;
  {
    const int sj = bid % 27, swh = (bid / 27) & 1, sq = min(bid / 54, 3), srg = tid >> 3;
    c.srdM = mk_srd(ws + WS_MOD, 2 * 6 * D * 4); c.srdS = mk_srd(swh ? wu : wg, D * F * 2); c.srdX = mk_srd(x, S * D * 2);
    const int t = min(tid, 479);
    c.voffM = 4 * (t < 80 ? 2 * D + nt * 80 + t : t < 160 ? 4 * D + nt * 80 + t - 80 : t < 320 ? 4 * D + sq * 160 + t - 160 : 3 * D + sq * 160 + t - 320);
    c.plus1 = t >= 80 && t < 320;
#pragma unroll
    for (int u = 0; u < 3; ++u) c.voffS[u] = srg + 64 * u < 160 ? ((sq * 160 + srg + 64 * u) * F + sj * 64 + (tid & 7) * 8) * 2 : 0x7fffffffu;
    c.voffX = (frow * D + nt * 80 + fch * 8) * 2;
    c.csdst = (bid < 216 && tid < 128) ? (float*)(ws + WS_CSBS) + (sj * 4 + sq) * 256 + swh * 128 + tid : nullptr;
  }
#pragma unroll
  for (int i = 0; i < 10; ++i) c.acc[i / 5][i % 5] = f32x4{};
  STAMP(1);
  asm volatile("s_waitcnt vmcnt(%2)\n\ts_barrier" : "+v"(of[0][0]), "+v"(of[0][1]) : "i"(k3_wait(0)) : "memory");
  STAMP(2);
#pragma unroll
  for (int cc = 0; cc < 5; ++cc) c.fw[cc] = k3_rd(c, cc);
  SBAR();
  k3_steps(c, of, std::make_integer_sequence<int, 32>{});
  STAMP(12);
  const int tile = rb * 8 + nt, own = w >> 1, g4 = lane >> 4, q16 = lane & 15;
  const __amdgpu_buffer_rsrc_t srdP = mk_srd(ws + WS_PART, 64 * 4 * 3 * 10240);
  if (own != kq) {
    const unsigned so = ((tile * 4 + own) * 3 + (kq - (kq > own))) * 10240 + (w & 1) * 5120;
    const unsigned vo = ((g4 & 1) * 2 + (q16 >> 3)) * 1280 + (g4 >> 1) * 128 + (q16 & 7) * 16;
#pragma unroll
    for (int cc = 0; cc < 5; ++cc) {
      const auto r0 = __builtin_amdgcn_permlane16_swap(pk2(c.acc[0][cc][0], c.acc[0][cc][1]), pk2(c.acc[1][cc][0], c.acc[1][cc][1]), false, false);
      const auto r1 = __builtin_amdgcn_permlane16_swap(pk2(c.acc[0][cc][2], c.acc[0][cc][3]), pk2(c.acc[1][cc][2], c.acc[1][cc][3]), false, false);
      __builtin_amdgcn_raw_buffer_store_b128((u32x4){r0[0], r1[0], r0[1], r1[1]}, srdP, vo + cc * 256, so, 16);
    }
    asm volatile("s_waitcnt vmcnt(0)" ::: "memory");
    if (lane == 0) __hip_atomic_fetch_add((gu32*)(ws + WS_CNT + (tile * 4 + own) * 64), 1u, __ATOMIC_RELAXED, __HIP_MEMORY_SCOPE_AGENT);
  } else {
#pragma unroll
    for (int cc = 0; cc < 5; ++cc)
#pragma unroll
      for (int r2 = 0; r2 < 2; ++r2) *(LAS f32x4*)(sm + K3_OWN + (((w & 1) * 32 + r2 * 16 + q16) * RLD + cc * 16 + g4 * 4) * 4) = c.acc[r2][cc];
  }
  STAMP(13);
  asm volatile("s_waitcnt vmcnt(1)" : "+v"(c.m0_), "+v"(c.m1_), "+v"(c.sw[0]), "+v"(c.sw[1]), "+v"(c.sw[2]), "+v"(c.xv[0]) :: "memory");
  { const float mv = rbf(c.m0_ + c.m1_); *(LAS float*)(sm + K3_MODL + tid * 4) = c.plus1 ? rbf(1.f + mv) : mv; }
  asm volatile("s_waitcnt lgkmcnt(0)\n\ts_barrier" ::: "memory");
  {
    const int srg = tid >> 3;
    float ac[8] = {0, 0, 0, 0, 0, 0, 0, 0}, ab[8] = {0, 0, 0, 0, 0, 0, 0, 0};
#pragma unroll
    for (int u = 0; u < 3; ++u) {
      const LAS float* mm = (const LAS float*)(sm + K3_MODL) + 160 + min(srg + 64 * u, 159);
      const u32x4 w4 = __builtin_bit_cast(u32x4, c.sw[u]);
      const float a = mm[0], sbv = mm[160];
#pragma unroll
      for (int e = 0; e < 4; ++e) { const float lo = bflo(w4[e]), hi = bfhi(w4[e]); ac[2 * e] += a * lo; ac[2 * e + 1] += a * hi; ab[2 * e] += sbv * lo; ab[2 * e + 1] += sbv * hi; }
    }
    const bool hi = tid & 8;
    float r[8];
#pragma unroll
    for (int e = 0; e < 8; ++e) {
      float v = (hi ? ab[e] : ac[e]) + xor8(hi ? ac[e] : ab[e]);
      { auto q = __builtin_amdgcn_permlane16_swap(__float_as_uint(v), __float_as_uint(v), false, false); v = __uint_as_float(q[0]) + __uint_as_float(q[1]); }
      { auto q = __builtin_amdgcn_permlane32_swap(__float_as_uint(v), __float_as_uint(v), false, false); v = __uint_as_float(q[0]) + __uint_as_float(q[1]); }
      r[e] = v;
    }
    LAS float* d = (LAS float*)(sm + K3_CSL) + w * 128 + (tid & 15) * 8;
    if (lane < 16) { *(LAS f32x4*)d = (f32x4){r[0], r[1], r[2], r[3]}; *(LAS f32x4*)(d + 4) = (f32x4){r[4], r[5], r[6], r[7]}; }
  }
  if (w == 2 * kq) {
    gu32* cnt = (gu32*)(ws + WS_CNT + (tile * 4 + kq) * 64);
    for (unsigned spins = 0; rfl(__hip_atomic_load(cnt, __ATOMIC_RELAXED, __HIP_MEMORY_SCOPE_AGENT)) < 6u && spins < (1u << 22); ++spins) __builtin_amdgcn_s_sleep(1);
  }
  asm volatile("s_waitcnt vmcnt(0) lgkmcnt(0)\n\ts_barrier" : "+v"(c.xv[1]) :: "memory");
  STAMP(14);
  u32x4 pv[2][3];
#pragma unroll
  for (int sI = 0; sI < 3; ++sI) {
    const unsigned so = ((tile * 4 + kq) * 3 + sI) * 10240 + w * 1280;
    pv[0][sI] = __builtin_amdgcn_raw_buffer_load_b128(srdP, c.voff, so, 16);
    pv[1][sI] = __builtin_amdgcn_raw_buffer_load_b128(srdP, 1024 + (lane & 15) * 16, so, 16);
  }
  if (c.csdst) { const LAS float* d = (const LAS float*)(sm + K3_CSL) + (tid & 127); *c.csdst = ((d[0] + d[128]) + (d[256] + d[384])) + ((d[512] + d[640]) + (d[768] + d[896])); }
  float v[2][8];
#pragma unroll
  for (int it = 0; it < 2; ++it) {
    const int ch = it ? 8 + (fch & 1) : fch;
    const LAS float* po = (const LAS float*)(sm + K3_OWN) + (w * 8 + (lane & 7)) * RLD + ch * 8;
    const LAS float* pm = (const LAS float*)(sm + K3_MODL) + ch * 8;
    f32x4 t[2] = {*(const LAS f32x4*)po, *(const LAS f32x4*)(po + 4)};
    const f32x4 ga[2] = {*(const LAS f32x4*)pm, *(const LAS f32x4*)(pm + 4)}, a1[2] = {*(const LAS f32x4*)(pm + 80), *(const LAS f32x4*)(pm + 84)};
    const u32x4 xx = __builtin_bit_cast(u32x4, c.xv[it]);
    u32x4 o, y;
#pragma unroll
    for (int e = 0; e < 4; ++e) {
      const float lo = t[e >> 1][2 * (e & 1)] + ((bflo(pv[it][0][e]) + bflo(pv[it][1][e])) + bflo(pv[it][2][e]));
      const float hi = t[e >> 1][2 * (e & 1) + 1] + ((bfhi(pv[it][0][e]) + bfhi(pv[it][1][e])) + bfhi(pv[it][2][e]));
      o[e] = pk2(bflo(xx[e]) + ga[e >> 1][2 * (e & 1)] * lo, bfhi(xx[e]) + ga[e >> 1][2 * (e & 1) + 1] * hi);
      v[it][2 * e] = bflo(o[e]); v[it][2 * e + 1] = bfhi(o[e]);
      y[e] = pk2(v[it][2 * e] * a1[e >> 1][2 * (e & 1)], v[it][2 * e + 1] * a1[e >> 1][2 * (e & 1) + 1]);
    }
    const int cg = nt * 10 + ch;
    const size_t toff = ((size_t)(frow >> 5) * 40 + (cg >> 1)) * 1024 + ((frow & 31) + 32 * (cg & 1)) * 16;
    if (it == 0 || lane < 16) { *(u32x4*)(ws + WS_Y + toff) = y; *(u32x4*)(ws + WS_X1 + toff) = o; }
  }
  {
    const float on = lane < 16 ? 1.f : 0.f;
    float s0 = 0.f, s1 = 0.f;
#pragma unroll
    for (int e = 0; e < 8; ++e) { s0 += v[0][e]; s1 += v[1][e]; }
    const float mu = sum816(lane < 16 ? s0 + s1 : s0) * (1.f / 80.f);
    float q0 = 0.f, q1 = 0.f;
#pragma unroll
    for (int e = 0; e < 8; ++e) { q0 += (v[0][e] - mu) * (v[0][e] - mu); q1 += (v[1][e] - mu) * (v[1][e] - mu); }
    const float m2 = sum816(lane < 16 ? q0 + q1 : q0);
    (void)on;
    if (lane < 8) *(f32x2*)(ws + WS_STAT + (size_t)frow * 64 + nt * 8) = (f32x2){mu, m2};
  }
  STAMP(15); STAMP_FLUSH(3, STAMP_WAVE);
}

constexpr int K5_MOD = 4 * 64 * RLD, LDS5 = (K5_MOD + 256) * 4;
__global__ __launch_bounds__(256, 1) void k5_down(const float* __restrict__ mod, const char* __restrict__ ws, u16* __restrict__ out) {
  extern __shared__ __attribute__((aligned(16))) char smem[];
  float* red = (float*)smem;
  const int tid = threadIdx.x, lane = tid & 63, bid = blockIdx.x;
  const int w = rfl(tid >> 6);
  const int rb = (bid & 7) * 4 + ((bid >> 3) & 3), nt = bid >> 5;
  STAMP_DECL; STAMP(0);
  constexpr int KS = 54, KW = 14;
  const int k0 = w < 2 ? w * KW : 2 * KW + (w - 2) * (KW - 1), nk = w < 2 ? KW : KW - 1;
  const char* pa = ws + WS_ACT + ((size_t)rb * KS + k0) * 4096 + lane * 16;
  const char* pw = ws + WS_WDT + ((size_t)nt * KS + k0) * 5120 + lane * 16;
  const int q = lane, row = rb * 64 + q;
  const int mo = 5 * D + min(tid, 79) + nt * 80;
  const float mv0 = mod[mo], mv1 = mod[6 * D + mo];
  SBAR();
  constexpr int PD = 3;
  bf16x8 fa[PD][4], fw[PD][5];
  f32x4 acc[4][5] = {};
#pragma unroll
  for (int i = 0; i < PD; ++i) {
#pragma unroll
    for (int r = 0; r < 4; ++r) fa[i][r] = *(const bf16x8*)(pa + i * 4096 + r * 1024);
#pragma unroll
    for (int cc = 0; cc < 5; ++cc) fw[i][cc] = *(const bf16x8*)(pw + i * 5120 + cc * 1024);
    SBAR();
  }
  red[K5_MOD + tid] = rbf(mv0 + mv1);
  SBAR();
  u32x4 xv[3];
#pragma unroll
  for (int i = 0; i < KW; ++i) {
    const int b = i % PD;
    if (i < KW - 1 || nk == KW) {
#pragma unroll
      for (int r = 0; r < 4; ++r)
#pragma unroll
        for (int cc = 0; cc < 5; ++cc) acc[r][cc] = MFMA16(fw[b][cc], fa[b][r], acc[r][cc]);
    }
    SBAR();
    if (i + PD < KW - 1 || (i + PD == KW - 1 && nk == KW)) {
#pragma unroll
      for (int r = 0; r < 4; ++r) fa[b][r] = *(const bf16x8*)(pa + (i + PD) * 4096 + r * 1024);
#pragma unroll
      for (int cc = 0; cc < 5; ++cc) fw[b][cc] = *(const bf16x8*)(pw + (i + PD) * 5120 + cc * 1024);
    }
    if (i == KW - PD) {
#pragma unroll
      for (int it = 0; it < 3; ++it) {
        const int ch = min(w + 4 * it, 9), n0 = nt * 80 + ch * 8;
        xv[it] = *(const u32x4*)(ws + WS_X1 + ((size_t)(row >> 5) * 40 + (n0 >> 4)) * 1024 + ((row & 31) + 32 * ((n0 >> 3) & 1)) * 16);
      }
    }
    SBAR();
  }
  STAMP(1);
#pragma unroll
  for (int r = 0; r < 4; ++r)
#pragma unroll
    for (int cc = 0; cc < 5; ++cc) *(f32x4*)(red + (w * 64 + r * 16 + (lane & 15)) * RLD + cc * 16 + (lane >> 4) * 4) = acc[r][cc];
  __syncthreads();
  STAMP(2);
#pragma unroll
  for (int it = 0; it < 3; ++it) {
    const int ch = w + 4 * it;
    if (ch < 10) {
      f32x4 s0 = {}, s1 = {};
#pragma unroll
      for (int ww = 0; ww < 4; ++ww) { s0 += *(const f32x4*)(red + (ww * 64 + q) * RLD + ch * 8); s1 += *(const f32x4*)(red + (ww * 64 + q) * RLD + ch * 8 + 4); }
      const f32x4 g0 = *(const f32x4*)(red + K5_MOD + ch * 8), g1 = *(const f32x4*)(red + K5_MOD + ch * 8 + 4);
      u32x4 o;
      o[0] = pk2(bflo(xv[it][0]) + g0[0] * s0[0], bfhi(xv[it][0]) + g0[1] * s0[1]); o[1] = pk2(bflo(xv[it][1]) + g0[2] * s0[2], bfhi(xv[it][1]) + g0[3] * s0[3]);
      o[2] = pk2(bflo(xv[it][2]) + g1[0] * s1[0], bfhi(xv[it][2]) + g1[1] * s1[1]); o[3] = pk2(bflo(xv[it][3]) + g1[2] * s1[2], bfhi(xv[it][3]) + g1[3] * s1[3]);
      *(u32x4*)(out + (size_t)row * D + nt * 80 + ch * 8) = o;
    }
  }
  STAMP(3); STAMP_FLUSH(5, 0);
}

struct K4C { __amdgpu_buffer_rsrc_t srdG, srdU, srdH; unsigned voffW, voffH, m0base, sofW, sofH, aW; const LAS char* pW; bf16x8 f[4]; f32x16 o[4]; LAS float* cs; int tid;
  __amdgpu_buffer_rsrc_t srdS; unsigned m0cs, sofcs, voffSt; bf16x8 st[4]; };
template <int I, int HALF> DI void k4_dma1(const K4C& c) { dma16(c.m0base + (I & (NSLOT - 1)) * SLOT + HALF * 1024, c.voffW, HALF ? c.srdU : c.srdG, c.sofW + I * 64 * F * 2); }
template <int I, int S_> DI void k4_ldh1(const K4C& c, bf16x8& r) { ld16<S_ * 1024>(r, c.voffH, c.srdH, c.sofH + I * 4096); }
template <int I> DI void k4_ld(const K4C& c, bf16x8 (&hf)[4]) { k4_ldh1<I, 0>(c, hf[0]); k4_ldh1<I, 1>(c, hf[1]); k4_ldh1<I, 2>(c, hf[2]); k4_ldh1<I, 3>(c, hf[3]); k4_dma1<I, 0>(c); k4_dma1<I, 1>(c); }
DI bf16x8 rdW4(const K4C& c, int g) { const LAS char* p = c.pW + (g >> 2) * 4096 + (g & 3) * 512; return lds_tr(p, p + 256); }
template <int I, int G> DI void k4_gap(K4C& c, bf16x8 (&hf)[10][4]) {
  c.o[G & 3] = MFMA32(c.f[G & 3], hf[I][G >> 2], c.o[G & 3]);
  if constexpr (G < 12) c.f[G & 3] = rdW4(c, G + 4);
  else if constexpr (I < 9) c.f[G & 3] = rdW4(c, G - 12);
  if constexpr (G == 11 && I < 9) c.pW = slot_ptr(c.aW, I + 1);
  if constexpr (I == 0) {
    if constexpr (G < 4) k4_ldh1<2, G>(c, hf[2][G]);
    if constexpr (G == 4 || G == 5) k4_dma1<2, (G == 5)>(c);
    if constexpr (G >= 9 && G < 13) k4_ldh1<3, G - 9>(c, hf[3][G - 9]);
    if constexpr (G == 13 || G == 14) k4_dma1<3, (G == 14)>(c);
  } else {
    if constexpr (G < 8 && (G & 1) && I + 3 < 10) k4_ldh1<I + 3, (G >> 1)>(c, hf[I + 3][G >> 1]);
    if constexpr ((G == 10 || G == 13) && I + 3 < 10) k4_dma1<I + 3, (G == 13)>(c);
  }
  if constexpr (I == 7 && G == 0) dma16(c.m0cs, c.voffH, c.srdS, c.sofcs);
  if constexpr (I == 7 && G >= 1 && G < 5) ld16<16 * (G - 1)>(c.st[G - 1], c.voffSt, c.srdS, WS_STAT);
  if constexpr (G == 8 && I < 9) wait_bar4v<(I == 0 ? 6 : I < 7 ? 10 : I == 7 ? 11 : 0)>(hf[I + 1][0], hf[I + 1][1], hf[I + 1][2], hf[I + 1][3]);
  if constexpr (I == 8 && G == 9) {
    const LAS f32x4* s4 = (const LAS f32x4*)c.cs + (c.tid & 63);
    *((LAS f32x4*)c.cs + 512 + c.tid) = (s4[0] + s4[64]) + (s4[128] + s4[192]);
  }
  SBAR();
}
template <int I> DI void k4_tile(K4C& c, bf16x8 (&hf)[10][4]) {
  k4_gap<I, 0>(c, hf); k4_gap<I, 1>(c, hf); k4_gap<I, 2>(c, hf); k4_gap<I, 3>(c, hf); k4_gap<I, 4>(c, hf); k4_gap<I, 5>(c, hf); k4_gap<I, 6>(c, hf); k4_gap<I, 7>(c, hf);
  k4_gap<I, 8>(c, hf); k4_gap<I, 9>(c, hf); k4_gap<I, 10>(c, hf); k4_gap<I, 11>(c, hf); k4_gap<I, 12>(c, hf); k4_gap<I, 13>(c, hf); k4_gap<I, 14>(c, hf); k4_gap<I, 15>(c, hf);
}
constexpr int LDS4 = NSLOT * SLOT + 8192 + 8192;
__global__ __launch_bounds__(512, 2) void k4_gateup(const u16* __restrict__ wg, const u16* __restrict__ wu, char* __restrict__ ws) {
  extern __shared__ __attribute__((aligned(16))) char smem[];
  const int tid = threadIdx.x, lane = tid & 63, bid = blockIdx.x;
  const unsigned w = rfl(tid >> 6);
  const int qb = bid & 7, j = bid >> 3, r32 = lane & 31, h = lane >> 5;
  const int qg = qb * 256 + w * 32 + r32;
  STAMP_DECL; STAMP(0);
  K4C c;
  c.srdG = mk_srd(wg, D * F * 2); c.srdU = mk_srd(wu, D * F * 2); c.srdH = mk_srd(ws + WS_Y, S * D * 2);
  const unsigned sb = (unsigned)(size_t)(LAS char*)smem;
  if (bid >= 216) {
    return;
  }
  {
    const unsigned row = (lane >> 2) & 7, sub = lane >> 5, ch = lane & 3;
    c.voffW = row * (F * 2) + sub * 64 + ch * 16; c.voffH = lane * 16; c.m0base = sb + w * 2048;
    c.sofW = w * 8 * (F * 2) + j * 128; c.sofH = (qb * 8 + w) * 40 * 1024;
    const int blk = (lane >> 4) & 1, q = (lane >> 2) & 3, p = lane & 3, sp = ((p & 1) << 1) | (p >> 1);
    c.aW = sb + h * 2048 + q * 64 + blk * 32 + sp * 8;
    c.cs = (LAS float*)((LAS char*)smem + NSLOT * SLOT); c.tid = tid;
    c.srdS = mk_srd(ws, WS_H); c.m0cs = sb + NSLOT * SLOT + w * 1024; c.sofcs = WS_CSBS + j * 4096 + (w & 3) * 1024; c.voffSt = qg * 64;
  }
  asm volatile("s_nop 4" ::: "memory");
  bf16x8 hf[10][4];
  k4_ld<0>(c, hf[0]);
  asm volatile("s_barrier" ::: "memory");
  k4_ld<1>(c, hf[1]);
#pragma unroll
  for (int dt = 0; dt < 4; ++dt) c.o[dt] = f32x16{};
  c.pW = slot_ptr(c.aW, 0);
  STAMP(1);
  wait_bar4v<6>(hf[0][0], hf[0][1], hf[0][2], hf[0][3]);
  STAMP(2);
#pragma unroll
  for (int g = 0; g < 4; ++g) c.f[g] = rdW4(c, g);
  SBAR();
  k4_tile<0>(c, hf); k4_tile<1>(c, hf); k4_tile<2>(c, hf); k4_tile<3>(c, hf); k4_tile<4>(c, hf);
  STAMP(3);
  k4_tile<5>(c, hf); k4_tile<6>(c, hf); k4_tile<7>(c, hf); k4_tile<8>(c, hf); k4_tile<9>(c, hf);
  STAMP(4);
  asm volatile("" : "+v"(c.st[0]), "+v"(c.st[1]), "+v"(c.st[2]), "+v"(c.st[3]));
  float mean = 0.f, m2 = 0.f;
  {
    f32x4 sv[4];
#pragma unroll
    for (int i = 0; i < 4; ++i) { sv[i] = __builtin_bit_cast(f32x4, c.st[i]); mean += sv[i][0] + sv[i][2]; }
    mean *= 0.125f;
#pragma unroll
    for (int i = 0; i < 4; ++i) m2 += sv[i][1] + sv[i][3] + 80.f * ((sv[i][0] - mean) * (sv[i][0] - mean) + (sv[i][2] - mean) * (sv[i][2] - mean));
  }
  const float rs = rsqrtf(m2 * (1.f / D) + EPS), nmr = -mean * rs;
  const LAS float* cb = c.cs + 2048 + w * 256;
  char* ob = ws + WS_ACT + ((size_t)((qg >> 6) * 54 + j * 2) * 4 + ((qg >> 4) & 3)) * 1024 + ((qg & 15) + 16 * h) * 16;
#pragma unroll
  for (int t = 0; t < 2; ++t)
#pragma unroll
    for (int s = 0; s < 2; ++s) {
      const int f0 = 32 * t + 16 * s + 8 * h;
      float a[8];
#pragma unroll
      for (int hh = 0; hh < 2; ++hh) {
        const f32x4 csg = *(const LAS f32x4*)(cb + f0 + 4 * hh), bsg = *(const LAS f32x4*)(cb + 64 + f0 + 4 * hh);
        const f32x4 csu = *(const LAS f32x4*)(cb + 128 + f0 + 4 * hh), bsu = *(const LAS f32x4*)(cb + 192 + f0 + 4 * hh);
#pragma unroll
        for (int e = 0; e < 4; ++e) {
          const int r = 8 * s + 4 * hh + e;
          const float g = rs * c.o[t][r] + (nmr * csg[e] + bsg[e]);
          const float u = rs * c.o[2 + t][r] + (nmr * csu[e] + bsu[e]);
          a[4 * hh + e] = g * u * __builtin_amdgcn_rcpf(1.f + __builtin_amdgcn_exp2f(-1.4426950408889634f * g));
        }
      }
      u32x4 o = {pk2(a[0], a[1]), pk2(a[2], a[3]), pk2(a[4], a[5]), pk2(a[6], a[7])};
      *(u32x4*)(ob + t * 4096 + s * 512) = o;
    }
  STAMP(5); STAMP_FLUSH(4, STAMP_WAVE);
}

extern "C" void kernel_launch(void* const* d_in, const int* in_sizes, int n_in, void* d_out, int out_size, void* d_ws, size_t ws_size, hipStream_t stream) {
  const u16* x = (const u16*)d_in[0]; const u16* kc = (const u16*)d_in[1]; const u16* vc = (const u16*)d_in[2];
  const u16* wq = (const u16*)d_in[3]; const u16* wo = (const u16*)d_in[4];
  const u16* wg = (const u16*)d_in[6]; const u16* wu = (const u16*)d_in[7]; const u16* wd = (const u16*)d_in[8];
  const u16* c = (const u16*)d_in[9]; const u16* wmod = (const u16*)d_in[10];
  char* ws = (char*)d_ws; u16* out = (u16*)d_out;
  const float* mod = (const float*)(ws + WS_MOD);
  constexpr int LDS2 = NSLOT * SLOT + 32768;
  (void)hipFuncSetAttribute((const void*)k2_attention, hipFuncAttributeMaxDynamicSharedMemorySize, LDS2);
  (void)hipFuncSetAttribute((const void*)k4_gateup, hipFuncAttributeMaxDynamicSharedMemorySize, LDS4);
  (void)hipFuncSetAttribute((const void*)k3_oproj, hipFuncAttributeMaxDynamicSharedMemorySize, LDS3);
  (void)hipFuncSetAttribute((const void*)k5_down, hipFuncAttributeMaxDynamicSharedMemorySize, LDS5);
  hipLaunchKernelGGL(k1_front, dim3(K1_LN + K1_MOD), dim3(256), 0, stream, x, c, wmod, ws);
  hipLaunchKernelGGL(k2_attention, dim3(256), dim3(512), LDS2, stream, wq, kc, vc, wo, wd, wg, wu, ws);
  hipLaunchKernelGGL(k3_oproj, dim3(256), dim3(512), LDS3, stream, x, wg, wu, ws);
  hipLaunchKernelGGL(k4_gateup, dim3(256), dim3(512), LDS4, stream, wg, wu, ws);
  hipLaunchKernelGGL(k5_down, dim3(256), dim3(256), LDS5, stream, mod, (const char*)ws, out);
}
```

```cpp
#include <hip/hip_runtime.h>
#include <hip/hip_bf16.h>
#include <stdint.h>
#include <utility>

typedef __bf16   bf16x8 __attribute__((ext_vector_type(8)));
typedef __bf16   bf16x4 __attribute__((ext_vector_type(4)));
typedef __bf16   bf16x2 __attribute__((ext_vector_type(2)));
typedef short    s16x4  __attribute__((ext_vector_type(4)));
typedef float    f32x16 __attribute__((ext_vector_type(16)));
typedef float    f32x4  __attribute__((ext_vector_type(4)));
typedef float    f32x2  __attribute__((ext_vector_type(2)));
typedef unsigned u32x4  __attribute__((ext_vector_type(4)));
typedef unsigned u32x2  __attribute__((ext_vector_type(2)));
typedef unsigned short u16;
#define LAS __attribute__((address_space(3)))
#define DI __device__ __forceinline__
#define SBAR() __builtin_amdgcn_sched_barrier(0)
#define MFMA32(a, b, c) __builtin_amdgcn_mfma_f32_32x32x16_bf16((a), (b), (c), 0, 0, 0)
#define MFMA16(a, b, c) __builtin_amdgcn_mfma_f32_16x16x32_bf16((a), (b), (c), 0, 0, 0)
#define MFMA16_ACC(c, a, b) asm volatile("v_mfma_f32_16x16x32_bf16 %0, %1, %2, %0" : "+v"(c) : "v"(a), "v"(b))

#define STAMP_DECL
#define STAMP(i)
#define STAMP_FLUSH(k, wv)

constexpr int S = 2048, D = 640, NH = 32, DH = 128, SK = 2048, W = 512, F = 1728, DQ = NH * DH;
constexpr float EPS = 1e-5f;

constexpr size_t WS_MOD  = 0;
constexpr size_t WS_LN   = 32768;
constexpr size_t WS_CSBS = 49152;
constexpr size_t WS_CNT  = 81920;
constexpr size_t WS_STAT = 196608;
constexpr size_t WS_H    = 327680;
constexpr size_t WS_XT   = WS_H + (size_t)S * D * 2;
constexpr size_t WS_Y    = WS_XT + (size_t)S * D * 2;
constexpr size_t WS_O    = WS_Y + (size_t)S * D * 2;
constexpr size_t WS_ACT  = WS_O + (size_t)S * DQ * 2;
constexpr size_t WS_WOT  = WS_ACT + (size_t)S * F * 2;
constexpr size_t WS_WDT  = WS_WOT + (size_t)DQ * D * 2;
constexpr size_t WS_WGU  = WS_WDT + (size_t)F * D * 2;
constexpr size_t WS_PART = WS_WGU + (size_t)F * D * 4;
constexpr int CNT_P = 0, CNT_Y = 256, CNT_A = 512, CNT_C = 768;
static_assert(WS_CSBS + 27 * 1024 <= WS_CNT && WS_CNT + 896 * 128 <= WS_STAT && WS_STAT + (size_t)S * 64 <= WS_H, "ws map");

DI float bf2f(u16 v) { return __uint_as_float((unsigned)v << 16); }
DI float bflo(unsigned v) { return __uint_as_float(v << 16); }
DI float bfhi(unsigned v) { return __uint_as_float(v & 0xffff0000u); }
DI unsigned pk2(float lo, float hi) { f32x2 v = {lo, hi}; bf16x2 b = __builtin_convertvector(v, bf16x2); return __builtin_bit_cast(unsigned, b); }
DI float rbf(float x) { return bflo(pk2(x, 0.f)); }
DI unsigned h_off(int row, int cg) { return (((row >> 5) * 20 + (cg >> 2)) * 2 + ((row >> 4) & 1)) * 1024 + ((row & 15) + 16 * (cg & 3)) * 16; }
DI unsigned rfl(unsigned x) { return __builtin_amdgcn_readfirstlane(x); }
DI int swap23(int i) { return (i & ~12) | ((i & 4) << 1) | ((i & 8) >> 1); }

DI void dma16(unsigned m0v, unsigned voff, __amdgpu_buffer_rsrc_t srd, unsigned soff) {
  asm volatile("s_mov_b32 m0, %0\n\ts_nop 0\n\tbuffer_load_dwordx4 %1, %2, %3 offen lds" :: "s"(m0v), "v"(voff), "s"(srd), "s"(soff) : "memory");
}
template <int MO, int SO> DI void dma16i(unsigned m0b, unsigned voff, __amdgpu_buffer_rsrc_t srd, unsigned sob) {
  unsigned t;
  asm volatile("s_add_u32 m0, %1, %5\n\ts_add_u32 %0, %4, %6\n\tbuffer_load_dwordx4 %2, %3, %0 offen lds" : "=&s"(t) : "s"(m0b), "v"(voff), "s"(srd), "s"(sob), "i"(MO), "i"(SO) : "memory", "scc");
}
template <int OFF> DI void ld16(bf16x8& r, unsigned voff, __amdgpu_buffer_rsrc_t srd, unsigned soff) {
  asm volatile("buffer_load_dwordx4 %0, %1, %2, %3 offen offset:%4" : "=v"(r) : "v"(voff), "s"(srd), "s"(soff), "i"(OFF) : "memory");
}
template <int OFF> DI void ld16c(bf16x8& r, unsigned voff, __amdgpu_buffer_rsrc_t srd, unsigned soff) {
#define SC1S " sc1"
  asm volatile("buffer_load_dwordx4 %0, %1, %2, %3 offen offset:%4" SC1S : "=v"(r) : "v"(voff), "s"(srd), "s"(soff), "i"(OFF) : "memory");
}
template <int AUX> DI void st16(u32x4 v, __amdgpu_buffer_rsrc_t srd, unsigned voff) { __builtin_amdgcn_raw_buffer_store_b128(v, srd, voff, 0, AUX); }
DI unsigned umax4(const u32x4& v, unsigned m) { return max(max(max(m, v[0]), v[1]), max(v[2], v[3])); }
DI unsigned umax4(const bf16x8& v, unsigned m) { return umax4(__builtin_bit_cast(u32x4, v), m); }
DI bool pending(unsigned m) { return __builtin_expect(__any(m == 0xffffffffu), 0); }
DI u32x4 nopoison(u32x4 v) { return (u32x4){min(v[0], 0xfffffffeu), min(v[1], 0xfffffffeu), min(v[2], 0xfffffffeu), min(v[3], 0xfffffffeu)}; }
constexpr unsigned SPIN_CAP = 1u << 16;
DI unsigned umax16(const bf16x8& a, const bf16x8& b, const bf16x8& c, const bf16x8& d) {
  const u32x4 p = __builtin_bit_cast(u32x4, a), q = __builtin_bit_cast(u32x4, b), r = __builtin_bit_cast(u32x4, c), t = __builtin_bit_cast(u32x4, d);
  auto m3 = [](unsigned x, unsigned y, unsigned z) { return max(max(x, y), z); };
  unsigned m = m3(p[0], p[1], p[2]), n = m3(p[3], q[0], q[1]);
  m = m3(m, q[2], q[3]); n = m3(n, r[0], r[1]); m = m3(m, r[2], r[3]); n = m3(n, t[0], t[1]); m = m3(m, t[2], t[3]);
  return max(m, n);
}
template <int STEP> DI void reload4(unsigned mx, bf16x8& a, bf16x8& b, bf16x8& c, bf16x8& d, unsigned voff, __amdgpu_buffer_rsrc_t srd, unsigned soff) {
  if (pending(mx)) {
    unsigned spins = 0;
    do asm volatile("buffer_load_dwordx4 %0, %4, %5, %6 offen sc1\n\tbuffer_load_dwordx4 %1, %4, %5, %6 offen offset:%7 sc1\n\t"
                    "buffer_load_dwordx4 %2, %4, %5, %6 offen offset:%8 sc1\n\tbuffer_load_dwordx4 %3, %4, %5, %6 offen offset:%9 sc1\n\ts_waitcnt vmcnt(0)"
                    : "+v"(a), "+v"(b), "+v"(c), "+v"(d) : "v"(voff), "s"(srd), "s"(soff), "i"(STEP), "i"(2 * STEP), "i"(3 * STEP) : "memory");
    while (__any(umax16(a, b, c, d) == 0xffffffffu) && ++spins < SPIN_CAP);
  }
}
DI void first8(bf16x8 (&a)[4], bf16x8 (&b)[4], unsigned voff, __amdgpu_buffer_rsrc_t srd, unsigned soa, unsigned sob) {
  unsigned spins = 0;
  do asm volatile("buffer_load_dwordx4 %0, %8, %9, %10 offen sc1\n\tbuffer_load_dwordx4 %1, %8, %9, %10 offen offset:1024 sc1\n\t"
                  "buffer_load_dwordx4 %2, %8, %9, %10 offen offset:2048 sc1\n\tbuffer_load_dwordx4 %3, %8, %9, %10 offen offset:3072 sc1\n\t"
                  "buffer_load_dwordx4 %4, %8, %9, %11 offen sc1\n\tbuffer_load_dwordx4 %5, %8, %9, %11 offen offset:1024 sc1\n\t"
                  "buffer_load_dwordx4 %6, %8, %9, %11 offen offset:2048 sc1\n\tbuffer_load_dwordx4 %7, %8, %9, %11 offen offset:3072 sc1\n\ts_waitcnt vmcnt(4)"
                  : "=&v"(a[0]), "=&v"(a[1]), "=&v"(a[2]), "=&v"(a[3]), "=&v"(b[0]), "=&v"(b[1]), "=&v"(b[2]), "=&v"(b[3]) : "v"(voff), "s"(srd), "s"(soa), "s"(sob) : "memory");
  while (__any(umax16(a[0], a[1], a[2], a[3]) == 0xffffffffu) && ++spins < SPIN_CAP);
}
template <int N> DI void wait_bar4(bf16x8& a, bf16x8& b, bf16x8& c, bf16x8& d) {
  asm volatile("s_waitcnt vmcnt(%4) lgkmcnt(0)\n\ts_barrier" : "+v"(a), "+v"(b), "+v"(c), "+v"(d) : "i"(N) : "memory");
}
template <int N> DI void wait_bar() { asm volatile("s_waitcnt vmcnt(%0) lgkmcnt(0)\n\ts_barrier" :: "i"(N) : "memory"); }
DI bf16x8 lds_rd(const LAS char* p) { return *(const LAS bf16x8*)p; }
DI bf16x8 lds_tr(const LAS char* p0, const LAS char* p1) {
  s16x4 lo = __builtin_amdgcn_ds_read_tr16_b64_v4i16((LAS s16x4*)p0);
  s16x4 hi = __builtin_amdgcn_ds_read_tr16_b64_v4i16((LAS s16x4*)p1);
  return __builtin_bit_cast(bf16x8, __builtin_shufflevector(lo, hi, 0, 1, 2, 3, 4, 5, 6, 7));
}
DI bf16x8 pack8(const f32x16& x, int s, float m) {
  u32x4 w = {pk2(x[8 * s] * m, x[8 * s + 1] * m), pk2(x[8 * s + 2] * m, x[8 * s + 3] * m), pk2(x[8 * s + 4] * m, x[8 * s + 5] * m), pk2(x[8 * s + 6] * m, x[8 * s + 7] * m)};
  return __builtin_bit_cast(bf16x8, w);
}
DI __amdgpu_buffer_rsrc_t mk_srd(const void* p, unsigned bytes) { return __builtin_amdgcn_make_buffer_rsrc((void*)p, (short)0, (int)bytes, 0x00020000); }

constexpr int K1_MOD = 120, K1_LN = 128;
__global__ __launch_bounds__(256) void k1_front(const u16* __restrict__ x, const u16* __restrict__ c, const u16* __restrict__ wmod, char* __restrict__ ws) {
  __shared__ float red[4][64];
  const int tid = threadIdx.x, bid = blockIdx.x, lane = tid & 63, wv = tid >> 6;
  STAMP_DECL; STAMP(0);
  if (bid < K1_MOD) {
    const int sl = bid >> 1, kh = bid & 1, ch = tid & 7, rg = tid >> 3;
    u32x4 wv4[10]; u16 cv[10];
#pragma unroll
    for (int i = 0; i < 10; ++i) wv4[i] = *(const u32x4*)(wmod + (size_t)(kh * 320 + rg + 32 * i) * (6 * D) + sl * 64 + ch * 8);
#pragma unroll
    for (int i = 0; i < 10; ++i) cv[i] = c[kh * 320 + rg + 32 * i];
    float acc[8] = {0, 0, 0, 0, 0, 0, 0, 0};
#pragma unroll
    for (int i = 0; i < 10; ++i) {
      const float a = bf2f(cv[i]), sv = rbf(a * __builtin_amdgcn_rcpf(1.f + __expf(-a)));
#pragma unroll
      for (int e = 0; e < 4; ++e) { acc[2 * e] += sv * bflo(wv4[i][e]); acc[2 * e + 1] += sv * bfhi(wv4[i][e]); }
    }
#pragma unroll
    for (int e = 0; e < 8; ++e) { acc[e] += __shfl_xor(acc[e], 8); acc[e] += __shfl_xor(acc[e], 16); acc[e] += __shfl_xor(acc[e], 32); }
    if (lane < 8) { *(f32x4*)&red[wv][ch * 8] = (f32x4){acc[0], acc[1], acc[2], acc[3]}; *(f32x4*)&red[wv][ch * 8 + 4] = (f32x4){acc[4], acc[5], acc[6], acc[7]}; }
    __syncthreads();
    if (tid < 64) ((float*)(ws + WS_MOD))[kh * 6 * D + sl * 64 + tid] = (red[0][tid] + red[1][tid]) + (red[2][tid] + red[3][tid]);
    STAMP(2); STAMP_FLUSH(1, 0);
    return;
  }
  if (tid < 7) *(unsigned*)(ws + WS_CNT + ((bid - K1_MOD) * 7 + tid) * 128) = 0u;
  const int row = (bid - K1_MOD) * 16 + wv * 4 + (lane >> 4), cg = lane & 15;
  u32x4 xv[5];
#pragma unroll
  for (int i = 0; i < 5; ++i) xv[i] = *(const u32x4*)(x + (size_t)row * D + (cg + 16 * i) * 8);
#pragma unroll
  for (int i = 0; i < 5; ++i) *(u32x4*)(ws + WS_H + (size_t)(bid - K1_MOD) * 20480 + i * 4096 + tid * 16) = (u32x4){~0u, ~0u, ~0u, ~0u};
#pragma unroll
  for (int i = 0; i < 5; ++i) *(u32x4*)(ws + WS_XT + h_off(row, cg + 16 * i)) = xv[i];
  float s = 0.f;
#pragma unroll
  for (int i = 0; i < 5; ++i)
#pragma unroll
    for (int e = 0; e < 4; ++e) s += bflo(xv[i][e]) + bfhi(xv[i][e]);
  s += __shfl_xor(s, 1); s += __shfl_xor(s, 2); s += __shfl_xor(s, 4); s += __shfl_xor(s, 8);
  const float m = s * (1.f / D);
  float q = 0.f;
#pragma unroll
  for (int i = 0; i < 5; ++i)
#pragma unroll
    for (int e = 0; e < 4; ++e) { const float a = bflo(xv[i][e]) - m, b = bfhi(xv[i][e]) - m; q += a * a + b * b; }
  q += __shfl_xor(q, 1); q += __shfl_xor(q, 2); q += __shfl_xor(q, 4); q += __shfl_xor(q, 8);
  if (cg == 0) *(f32x2*)(ws + WS_LN + row * 8) = (f32x2){m, rsqrtf(q * (1.f / D) + EPS)};
  STAMP(1); STAMP_FLUSH(1, 0);
}

constexpr int SLOT = 16384, NSLOT = 8;
constexpr float QSCALE = 0.08838834764831845f * 1.4426950408889634f;
constexpr float RTHR = 64.f;

#define PIN(x) asm volatile("" : "+v"(x))
#define EX2(x) __builtin_amdgcn_exp2f(x)
template <int N> DI void wait_bar4v(bf16x8& a, bf16x8& b, bf16x8& c, bf16x8& d) {
  asm volatile("s_waitcnt vmcnt(%4)\n\ts_barrier" : "+v"(a), "+v"(b), "+v"(c), "+v"(d) : "i"(N) : "memory");
}
template <int N> DI void wait_barv() { asm volatile("s_waitcnt vmcnt(%0)\n\ts_barrier" :: "i"(N) : "memory"); }
DI const LAS char* slot_ptr(unsigned lanebase, int tile) { unsigned a = lanebase + (tile & (NSLOT - 1)) * SLOT; PIN(a); return (const LAS char*)(size_t)a; }
DI float sum1632(float v) {
  { auto r = __builtin_amdgcn_permlane16_swap(__float_as_uint(v), __float_as_uint(v), false, false); v = __uint_as_float(r[0]) + __uint_as_float(r[1]); }
  { auto r = __builtin_amdgcn_permlane32_swap(__float_as_uint(v), __float_as_uint(v), false, false); v = __uint_as_float(r[0]) + __uint_as_float(r[1]); }
  return v;
}
DI float max1632(float v) {
  { auto r = __builtin_amdgcn_permlane16_swap(__float_as_uint(v), __float_as_uint(v), false, false); v = __builtin_fmaxf(__uint_as_float(r[0]), __uint_as_float(r[1])); }
  { auto r = __builtin_amdgcn_permlane32_swap(__float_as_uint(v), __float_as_uint(v), false, false); v = __builtin_fmaxf(__uint_as_float(r[0]), __uint_as_float(r[1])); }
  return v;
}
DI bf16x8 pack44(const f32x4& a, const f32x4& b, float m) { return __builtin_bit_cast(bf16x8, (u32x4){pk2(a[0] * m, a[1] * m), pk2(a[2] * m, a[3] * m), pk2(b[0] * m, b[1] * m), pk2(b[2] * m, b[3] * m)}); }

constexpr int S2 = 16416, NS2 = 7, L2_SIDE = NS2 * S2;
struct K2C {
  __amdgpu_buffer_rsrc_t srdW, srdK, srdV, srdH;
  unsigned voffW, voffK, voffV, voffH, m0T, m0K, sofW, sofKV, sofH, aT, aK;
  const LAS char *pT, *pK;
  bf16x8 f[3][4];
  bf16x8 qf[4][2];
  f32x4 o[8][2], sA[2][2], sB[2][2], negm[2];
  u32x4 pA[2], pB[2];
  float l[2], fp[2], aone; int pend, mx[2]; unsigned chk;
  __amdgpu_buffer_rsrc_t srdSs, srdSd;
  __amdgpu_buffer_rsrc_t srdGs, srdGd; unsigned voffG, sofGs, voffGd; const LAS char* pG;
  __amdgpu_buffer_rsrc_t srdT; unsigned sofT2;
  unsigned voffS, sofSs, sofSd[4], m0S; const LAS char* pS; bf16x8 sf[2];
};
DI const LAS char* ring_ptr(unsigned lanebase, int tile) { unsigned a = lanebase + (tile % NS2) * S2; PIN(a); return (const LAS char*)(size_t)a; }
template <int I, int HALF> DI void k2_dma1(const K2C& c) {
  constexpr int o = (I % NS2) * S2 + HALF * 1024;
  if constexpr (I < 10) dma16i<o, I * 64 * 8192 + HALF * 128>(c.m0T, c.voffW, c.srdW, c.sofW);
  else if constexpr (((I - 10) & 1) == 0) dma16i<o, ((I - 10) >> 1) * 16384 + HALF * 128>(c.m0K, c.voffK, c.srdK, c.sofKV);
  else dma16i<o, ((I - 10) >> 1) * 16384 + HALF * 128>(c.m0T, c.voffV, c.srdV, c.sofKV);
}
template <int I> DI void k2_dma(const K2C& c) { k2_dma1<I, 0>(c); k2_dma1<I, 1>(c); }
template <int I, int S_> DI void k2_ldh1(const K2C& c, bf16x8& r) { ld16c<S_ * 1024>(r, c.voffH, c.srdH, c.sofH + I * 4096); }
template <int H> DI void rdT(bf16x8& f, const LAS char* b, int ks, int dt) {
  const s16x4 v = __builtin_amdgcn_ds_read_tr16_b64_v4i16((LAS s16x4*)(b + ks * 8208 + (dt >> 1) * 512 + (dt & 1) * 8 + H * 256));
  u32x4 t = H ? __builtin_bit_cast(u32x4, f) : u32x4{}; const u32x2 x = __builtin_bit_cast(u32x2, v);
  if constexpr (H == 0) t = __builtin_shufflevector(x, x, 0, 1, -1, -1); else { t[2] = x[0]; t[3] = x[1]; }
  f = __builtin_bit_cast(bf16x8, t);
}
struct Quad { int kind, j, i; };
constexpr Quad quad_of(int Q) {
  if (Q < 40) return {0, Q >> 2, Q & 3};
  if (Q < 42) return {1, 0, Q - 40};
  const int T = (Q - 42) >> 3, r = (Q - 42) & 7;
  if (T < 7) return (r >> 1) == 0 ? Quad{1, 2 * T + 1, r & 1} : (r >> 1) == 1 ? Quad{2, 2 * T, r & 1} : (r >> 1) == 2 ? Quad{1, 2 * T + 2, r & 1} : Quad{2, 2 * T + 1, r & 1};
  return r < 2 ? Quad{1, 15, r} : r < 4 ? Quad{2, 14, r - 2} : r < 6 ? Quad{2, 15, r - 4} : Quad{3, 0, 0};
}
template <int Q, int M> DI void frag_ld(K2C& c) {
  constexpr Quad q = quad_of(Q); constexpr int E = M >> 1;
  if constexpr (M == 0 && q.i == 0) {
    if constexpr (q.kind == 0) c.pT = ring_ptr(c.aT, q.j);
    if constexpr (q.kind == 1 && (q.j & 1) == 0) c.pK = ring_ptr(c.aK, 10 + q.j);
    if constexpr (q.kind == 2 && (q.j & 1) == 0) c.pT = ring_ptr(c.aT, 11 + q.j);
  }
  if constexpr (q.kind == 0) rdT<(M & 1)>(c.f[Q % 3][E], c.pT, E & 1, 2 * q.i + (E >> 1));
  if constexpr (q.kind == 1 && (M & 1) == 0) c.f[Q % 3][E] = lds_rd(c.pK + (q.j & 1) * 8192 + q.i * 256 + E * 512);
  if constexpr (q.kind == 2) rdT<(M & 1)>(c.f[Q % 3][E], c.pT, q.j & 1, 4 * q.i + E);
}
template <int Q, int... Ms> DI void frag_ld_all(K2C& c, std::integer_sequence<int, Ms...>) { (frag_ld<Q, Ms>(c), ...); }
DI int imx3(int a, float b, float c) { return max(max(a, __float_as_int(b)), __float_as_int(c)); }
template <int U, int PART> DI void sm_max(K2C& c, f32x4 (&s)[2][2]) {
  if constexpr (PART == 0) c.mx[U] = imx3(imx3(__float_as_int(s[0][U][0]), s[0][U][1], s[0][U][2]), s[0][U][3], s[1][U][0]);
  else c.mx[U] = max(imx3(c.mx[U], s[1][U][1], s[1][U][2]), __float_as_int(s[1][U][3]));
  PIN(c.mx[U]);
}
template <int E> DI void sm_exp(f32x4 (&s)[2][2]) { float x = s[E >> 3][(E >> 2) & 1][E & 3]; asm volatile("v_exp_f32 %0, %0" : "+v"(x)); s[E >> 3][(E >> 2) & 1][E & 3] = x; }
template <int E> DI void sm_add(K2C& c, f32x4 (&s)[2][2]) { asm volatile("v_add_f32 %0, %0, %1" : "+v"(c.l[(E >> 2) & 1]) : "v"(s[E >> 3][(E >> 2) & 1][E & 3])); }
template <int V> DI void sm_first(K2C& c, f32x4 (&s)[2][2]) {
  if constexpr (V == 1 || V == 2) sm_max<1, V - 1>(c, s);
  if constexpr (V == 3) {
    float t0, t1;
#define RMOVE(MX, F, L, S0, S1, NG) \
                 "v_mov_b32 %[t0], %[" MX "]\n\tv_mov_b32 %[t1], %[" MX "]\n\ts_nop 1\n\tv_permlane16_swap_b32 %[t0], %[t1]\n\ts_nop 1\n\tv_max_i32 %[t0], %[t0], %[t1]\n\t" \
                 "v_mov_b32 %[t1], %[t0]\n\ts_nop 1\n\tv_permlane32_swap_b32 %[t0], %[t1]\n\ts_nop 1\n\tv_max3_i32 %[t0], %[t0], %[t1], 0\n\t" \
                 "v_exp_f32 %[" F "], -%[t0]\n\tv_sub_f32 %[t1], 0, %[t0]\n\ts_nop 1\n\tv_mul_f32 %[" L "], %[" L "], %[" F "]\n\t" \
                 "v_mfma_f32_16x16x4_f32 %[" S0 "], %[one], %[t1], %[" S0 "]\n\tv_mfma_f32_16x16x4_f32 %[" S1 "], %[one], %[t1], %[" S1 "]\n\tv_mfma_f32_16x16x4_f32 %[" NG "], %[one], %[t1], %[" NG "]\n\t"
    asm volatile("v_max_i32 %[t0], %[m0], %[m1]\n\ts_mov_b32 %[pend], 0\n\tv_cmp_lt_i32 vcc, %[thr], %[t0]\n\ts_cbranch_vccz 1f\n\t"
                 RMOVE("m0", "f0", "l0", "s00", "s10", "n0") RMOVE("m1", "f1", "l1", "s01", "s11", "n1")
                 "s_mov_b32 %[pend], 1\n\ts_nop 15\n1:"
                 : [s00] "+v"(s[0][0]), [s01] "+v"(s[0][1]), [s10] "+v"(s[1][0]), [s11] "+v"(s[1][1]), [n0] "+v"(c.negm[0]), [n1] "+v"(c.negm[1]),
                   [l0] "+v"(c.l[0]), [l1] "+v"(c.l[1]), [f0] "+v"(c.fp[0]), [f1] "+v"(c.fp[1]), [pend] "=&s"(c.pend), [t0] "=&v"(t0), [t1] "=&v"(t1)
                 : [m0] "v"(c.mx[0]), [m1] "v"(c.mx[1]), [thr] "s"(__float_as_int(RTHR)), [one] "v"(c.aone) : "vcc", "memory");
#undef RMOVE
  }
  if constexpr (V >= 6) sm_add<V - 6>(c, s);
  if constexpr (V >= 4) sm_exp<V - 4>(s);
}
template <int V> DI void sm_second(K2C& c, f32x4 (&s)[2][2], u32x4 (&p)[2], f32x4 (&n)[2][2], bool next) {
  if constexpr (V < 6) sm_add<10 + V>(c, s);
  if constexpr (V < 4) sm_exp<12 + V>(s);
  if constexpr (V >= 4 && V < 12) { constexpr int e = V - 4, kt = e >> 2, u = (e >> 1) & 1; unsigned r;
    asm volatile("v_cvt_pk_bf16_f32 %0, %1, %2" : "=v"(r) : "v"(s[kt][u][2 * (e & 1)]), "v"(s[kt][u][2 * (e & 1) + 1])); p[u][2 * kt + (e & 1)] = r; }
  if constexpr (V >= 14) { if (next) sm_max<0, V - 14>(c, n); }
}
DI void apply_pend(K2C& c) {
  asm volatile("" ::: "memory");
  if (__builtin_expect(c.pend != 0, 0)) {
    asm volatile("s_nop 7" : "+v"(c.o[6][0]), "+v"(c.o[6][1]), "+v"(c.o[7][0]), "+v"(c.o[7][1]), "+v"(c.o[5][0]), "+v"(c.o[5][1]), "+v"(c.o[4][0]), "+v"(c.o[4][1]));
#pragma unroll
    for (int dt = 0; dt < 8; ++dt) { c.o[dt][0] *= c.fp[0]; c.o[dt][1] *= c.fp[1]; }
  }
}
constexpr int bq_wait(int i) { return i == 0 ? 4 : i < 8 ? 10 : 8; }
constexpr int ba_wait(int t) { return t == 0 ? 6 : t <= 3 ? 8 : t == 4 ? 12 : t == 5 ? 8 : t == 6 ? 6 : 9; }
template <int P> DI void side_ld(const K2C& c) { dma16i<P * 1024, P * 8 * D * 2>(c.m0S, c.voffS, c.srdSs, c.sofSs); }
template <int P> DI void side_rd(K2C& c) { const LAS char* p = c.pS + (P >> 1) * 512 + (P & 1) * 32; c.sf[P & 1] = lds_tr(p, p + 256); }
template <int P> DI void side_st(const K2C& c) { st16<16>(__builtin_bit_cast(u32x4, c.sf[P & 1]), c.srdSd, c.voffH + c.sofSd[P]); }
template <int P> DI void side2_ld(const K2C& c) { dma16i<P * 1024, P * 8 * F * 2>(c.m0S, c.voffG, c.srdGs, c.sofGs); }
template <int P> DI void side2_rd(K2C& c) { const LAS char* p = c.pG + (P >> 1) * 512 + (P & 1) * 8; c.sf[P & 1] = lds_tr(p, p + 256); }
template <int P> DI void side2_st(const K2C& c) { st16<16>(__builtin_bit_cast(u32x4, c.sf[P & 1]), c.srdGd, c.voffGd + (P >> 1) * 8192 + (P & 1) * 2048); }
template <int Q, int M> DI void k2_mem(K2C& c, bf16x8 (&hf)[10][4]) {
  if constexpr (Q < 40) {
    constexpr int I = Q >> 2, X = (Q & 3) * 8 + M;
    if constexpr (I == 0) { if constexpr (X < 16 && (X & 3) == 1) k2_ldh1<2, (X >> 2)>(c, hf[2][X >> 2]); if constexpr (X >= 16 && (X & 3) == 1) k2_ldh1<3, (X >> 2) - 4>(c, hf[3][(X >> 2) - 4]);
                             if constexpr (X == 27 || X == 31) k2_dma1<3, (X == 31)>(c); }
    else {
      if constexpr (X < 16 && (X & 3) == 1) {
        if constexpr (I < 7) k2_ldh1<I + 3, (X >> 2)>(c, hf[I + 3][X >> 2]);
        else if constexpr (I == 7) k2_dma1<10 + (X >> 3), (X >> 2) & 1>(c);
        else if constexpr (I == 8 && X < 8) k2_dma1<13, (X >> 2)>(c);
      }
      if constexpr (X == 19 || X == 27) k2_dma1<(I < 7 ? I + 3 : I == 7 ? 12 : I + 6), (X == 27)>(c);
    }
  } else if constexpr (Q >= 42) {
    constexpr int T = (Q - 42) >> 3, X = ((Q - 42) & 7) * 8 + M;
    if constexpr (X == 9 || X == 12) { if constexpr (T == 0) k2_dma1<16, (X == 12)>(c); else if constexpr (T <= 5) k2_dma1<15 + 2 * T, (X == 12)>(c); }
    if constexpr ((X == 13 || X == 16) && T >= 1 && T <= 4) k2_dma1<16 + 2 * T, (X == 16)>(c);
    if constexpr (T == 0 && (X == 41 || X == 44 || X == 45 || X == 48)) side_ld<(X == 41 ? 0 : X == 44 ? 1 : X == 45 ? 2 : 3)>(c);
    if constexpr (T == 2) {
      if constexpr (X == 43) { asm volatile("s_waitcnt vmcnt(8)" ::: "memory"); side_rd<0>(c); }
      if constexpr (X == 45) side_rd<1>(c);
      if constexpr (X == 49) { side_st<0>(c); side_rd<2>(c); }
      if constexpr (X == 51) { side_st<1>(c); side_rd<3>(c); }
      if constexpr (X == 57) side_st<2>(c);
      if constexpr (X == 59) side_st<3>(c);
    }
    if constexpr (T == 3 && (X == 41 || X == 44 || X == 45 || X == 48)) side2_ld<(X == 41 ? 0 : X == 44 ? 1 : X == 45 ? 2 : 3)>(c);
    if constexpr (T == 5) {
      if constexpr (X == 43) { asm volatile("s_waitcnt vmcnt(6)" ::: "memory"); side2_rd<0>(c); }
      if constexpr (X == 45) side2_rd<1>(c);
      if constexpr (X == 49) { side2_st<0>(c); side2_rd<2>(c); }
      if constexpr (X == 51) { side2_st<1>(c); side2_rd<3>(c); }
      if constexpr (X == 57) side2_st<2>(c);
      if constexpr (X == 59) side2_st<3>(c);
    }
    if constexpr (T == 6 && (X == 12 || X == 13 || X == 16 || X == 44 || X == 45)) dma16i<0, (X == 12 ? 0 : X == 13 ? 1 : X == 16 ? 2 : X - 41) * 1024>(c.m0S, c.voffH, c.srdT, c.sofT2);
  }
}
template <int SI, int M> DI void q_pack(K2C& c) {
  constexpr int t = M >> 2, u = (M >> 1) & 1, e = M & 1; unsigned r;
  asm volatile("v_cvt_pk_bf16_f32 %0, %1, %2" : "=v"(r) : "v"(c.o[2 * SI + t][u][2 * e]), "v"(c.o[2 * SI + t][u][2 * e + 1]));
  u32x4 v = __builtin_bit_cast(u32x4, c.qf[SI][u]); v[2 * t + e] = r; c.qf[SI][u] = __builtin_bit_cast(bf16x8, v);
}
template <int... Ms> DI void q_pack_last(K2C& c, std::integer_sequence<int, Ms...>) { (q_pack<3, Ms>(c), ...); }
template <int Q, int M> DI void k2_slot(K2C& c, bf16x8 (&hf)[10][4]) {
  constexpr Quad q = quad_of(Q);
  bf16x8 (&f)[4] = c.f[Q % 3];
  if constexpr (q.kind == 0) { constexpr int dt = 2 * q.i + (M >> 2), u = (M >> 1) & 1, ks = M & 1; MFMA16_ACC(c.o[dt][u], f[2 * (M >> 2) + ks], hf[q.j][2 * ks + u]); }
  if constexpr (q.kind == 1) { constexpr int u = M >> 2, e = M & 3; f32x4& s = (q.j & 1) ? c.sB[q.i][u] : c.sA[q.i][u];
    if constexpr (e == 0) asm volatile("v_mfma_f32_16x16x32_bf16 %0, %1, %2, %3" : "=&v"(s) : "v"(f[0]), "v"(c.qf[0][u]), "v"(c.negm[u])); else MFMA16_ACC(s, f[e], c.qf[e][u]); }
  if constexpr (q.kind == 2) { constexpr int e = M >> 1, u = M & 1;
    if constexpr (q.j == 0) asm volatile("v_mfma_f32_16x16x32_bf16 %0, %1, %2, 0" : "=&v"(c.o[4 * q.i + e][u]) : "v"(f[e]), "v"(c.pA[u]));
    else MFMA16_ACC(c.o[4 * q.i + e][u], f[e], (q.j & 1) ? c.pB[u] : c.pA[u]); }
  SBAR();
  frag_ld<Q + 2, M>(c);
  k2_mem<Q, M>(c, hf);
  if constexpr (q.kind == 0 && q.i == 2 && q.j < 9) {
    const u32x4 t = __builtin_bit_cast(u32x4, hf[q.j + 1][M >> 1]);
    if constexpr (M == 0) asm volatile("v_max_u32 %0, %1, %2" : "=v"(c.chk) : "v"(t[0]), "v"(t[1]));
    else asm volatile("v_max3_u32 %0, %0, %1, %2" : "+v"(c.chk) : "v"(t[2 * (M & 1)]), "v"(t[2 * (M & 1) + 1]));
  }
  if constexpr (Q >= 37 && Q < 40) q_pack<Q - 37, M>(c);
  if constexpr (Q >= 42) {
    if constexpr (q.kind == 1) { if constexpr (q.j & 1) sm_second<8 * q.i + M>(c, c.sA, c.pA, c.sB, true); else sm_second<8 * q.i + M>(c, c.sB, c.pB, c.sA, true); }
    if constexpr (q.kind == 2 && q.j < 15) { if constexpr (q.j & 1) sm_first<8 * q.i + M>(c, c.sA); else sm_first<8 * q.i + M>(c, c.sB); }
  }
  SBAR();
}
template <int... Vs> DI void sm_second_all(K2C& c, std::integer_sequence<int, Vs...>) { (sm_second<Vs>(c, c.sB, c.pB, c.sA, false), ...); }
#define PR1 3
#define PR2 6
#define PR3 7
template <int Q> DI void k2_quad(K2C& c, bf16x8 (&hf)[10][4]) {
  constexpr Quad q = quad_of(Q);
  if constexpr (q.kind == 0 && q.i == 2) {
    if constexpr (q.j < 9) wait_bar4v<bq_wait(q.j)>(hf[q.j + 1][0], hf[q.j + 1][1], hf[q.j + 1][2], hf[q.j + 1][3]);
    else wait_barv<bq_wait(9)>();
  }
  if constexpr (q.kind == 0 && q.i == 3 && q.j < 9) {
    asm volatile("" ::: "memory");
    reload4<1024>(c.chk, hf[q.j + 1][0], hf[q.j + 1][1], hf[q.j + 1][2], hf[q.j + 1][3], c.voffH, c.srdH, c.sofH + (q.j + 1) * 4096);
  }
  if constexpr (q.kind == 1 && (q.j & 1) && q.i == 0) wait_barv<ba_wait(q.j >> 1)>();
  if constexpr (q.kind == 2 && q.j == 15 && q.i == 0) { sm_second_all(c, std::make_integer_sequence<int, 16>{}); asm volatile("s_nop 1"); }
  if constexpr (Q < 40) __builtin_amdgcn_s_setprio(3 - ((Q + 2) & 3));
  else if constexpr (Q >= 42) { constexpr int r = (Q - 42) & 7; if constexpr (r == 0) __builtin_amdgcn_s_setprio(3); if constexpr (r == PR1) __builtin_amdgcn_s_setprio(2);
                                if constexpr (r == PR2) __builtin_amdgcn_s_setprio(1); if constexpr (r == PR3) __builtin_amdgcn_s_setprio(0); }
  asm volatile("" :: "v"(c.f[Q % 3][0]), "v"(c.f[Q % 3][1]), "v"(c.f[Q % 3][2]), "v"(c.f[Q % 3][3]));
  SBAR();
  k2_slot<Q, 0>(c, hf); k2_slot<Q, 1>(c, hf); k2_slot<Q, 2>(c, hf); k2_slot<Q, 3>(c, hf); k2_slot<Q, 4>(c, hf); k2_slot<Q, 5>(c, hf); k2_slot<Q, 6>(c, hf); k2_slot<Q, 7>(c, hf);
  if constexpr (q.kind == 2 && q.i == 1 && q.j < 15) apply_pend(c);
}
template <int Q0, int... Qs> DI void k2_quads(K2C& c, bf16x8 (&hf)[10][4], std::integer_sequence<int, Qs...>) { (k2_quad<Q0 + Qs>(c, hf), ...); }

__global__ __launch_bounds__(512, 2) void k2_attention(const u16* __restrict__ wq, const u16* __restrict__ kc, const u16* __restrict__ vc,
                                                        const u16* __restrict__ wo, const u16* __restrict__ wd, const u16* __restrict__ wg, const u16* __restrict__ wu, char* __restrict__ ws) {
  extern __shared__ __attribute__((aligned(16))) char smem[];
  const int tid = threadIdx.x, lane = tid & 63, bid = blockIdx.x;
  const unsigned w = rfl(tid >> 6);
  const int head = ((bid & 7) >> 1) * 8 + ((bid >> 3) & 7), qb = (bid & 1) * 4 + (bid >> 6);
  STAMP_DECL; STAMP(0);
  const int hrow = qb * 256 + head * 8 + (lane & 7), hch = lane >> 3;
  const unsigned ho0 = h_off(hrow, w * 10 + hch), ho1 = lane < 16 ? h_off(hrow, w * 10 + 8 + hch) : 0x7fffffffu;
  u32x2 m0_, m1_, m2_, m3_, ln; bf16x8 xv[2];
  {
    const __amdgpu_buffer_rsrc_t srdM = mk_srd(ws, WS_Y);
    const unsigned vo = (w * 40 + min(lane, 39)) * 8;
    asm volatile("s_nop 4\n\tbuffer_load_dwordx2 %0, %7, %8, 0 offen\n\tbuffer_load_dwordx2 %1, %7, %8, 0 offen offset:2560\n\t"
                 "buffer_load_dwordx2 %2, %7, %8, %9 offen\n\tbuffer_load_dwordx2 %3, %7, %8, %9 offen offset:2560\n\tbuffer_load_dwordx2 %4, %10, %8, %11 offen\n\t"
                 "buffer_load_dwordx4 %5, %12, %8, %14 offen\n\tbuffer_load_dwordx4 %6, %13, %8, %14 offen\n\ts_barrier"
                 : "=&v"(m0_), "=&v"(m1_), "=&v"(m2_), "=&v"(m3_), "=&v"(ln), "=&v"(xv[0]), "=&v"(xv[1])
                 : "v"(vo), "s"(srdM), "s"(6 * D * 4), "v"(hrow * 8), "s"((unsigned)WS_LN), "v"(ho0), "v"(ho1), "s"((unsigned)WS_XT) : "memory");
  }
  SBAR();
  K2C c;
  const unsigned row = (lane >> 2) & 7, sub = lane >> 5, ch4 = lane & 3, sb = (unsigned)(size_t)(LAS char*)smem;
  c.srdW = mk_srd(wq, D * DQ * 2); c.voffW = row * 8192 + sub * 64 + ch4 * 16; c.m0T = sb + (w >> 1) * 4104 + (w & 1) * 2056; c.sofW = w * 8 * 8192 + head * 256;
  k2_dma<0>(c); k2_dma<1>(c); k2_dma<2>(c);
  SBAR();
  c.srdK = mk_srd(kc, NH * SK * DH * 2); c.srdV = mk_srd(vc, NH * SK * DH * 2); c.srdH = mk_srd(ws + WS_H, S * D * 2);
  {
    c.voffV = row * 256 + sub * 64 + ch4 * 16;
    c.voffK = row * 256 + sub * 64 + ((ch4 ^ (w & 2)) * 16);
    c.voffH = lane * 16;
    c.m0K = sb + w * 2048;
    c.sofKV = (head * SK + (SK - W) + w * 8) * 256;
    c.sofH = (qb * 8 + w) * 40 * 1024;
    const int g = lane >> 4, q = (lane >> 2) & 3, p = lane & 3;
    c.aT = sb + (g >> 1) * 4104 + (g & 1) * 2056 + q * 64 + p * 16;
    c.aK = sb + 2048 * q + 64 * p + 16 * (g ^ (q & 2));
    const unsigned u = bid * 8 + w; const bool isO = u < 1280, live = u < 1820;
    const unsigned uu = isO ? u : u - 1280, ks = uu / 10, nb = uu - ks * 10, KSn = isO ? 128 : 54;
    c.srdSs = mk_srd(isO ? wo : wd, live ? (isO ? DQ * D * 2 : F * D * 2) : 0);
    c.srdSd = mk_srd(ws + (isO ? WS_WOT : WS_WDT), live ? (isO ? DQ * D * 2 : F * D * 2) : 0);
    c.voffS = row * (D * 2) + sub * 64 + ch4 * 16;
    c.sofSs = ks * 32 * (D * 2) + nb * 128;
#pragma unroll
    for (int i = 0; i < 4; ++i) { const unsigned C = 4 * nb + i, nt = C / 5; c.sofSd[i] = ((nt * KSn + ks) * 5 + (C - nt * 5)) * 1024; }
    {
      const unsigned m = u >= 540, r = u - m * 540, k2 = r / 27, jg = r - k2 * 27; const bool on = u < 1080;
      c.srdGs = mk_srd(m ? wu : wg, on ? D * F * 2 : 0); c.srdGd = mk_srd(ws + WS_WGU, on ? D * F * 4 : 0);
      c.voffG = row * (F * 2) + sub * 64 + ch4 * 16; c.sofGs = k2 * 32 * (F * 2) + jg * 128;
      c.voffGd = ((jg * 10 + (k2 >> 1)) * 16 + m * 4 + (k2 & 1)) * 1024 + lane * 16;
      c.pG = (const LAS char*)(size_t)(sb + L2_SIDE + w * 4096 + g * 1024 + q * 64 + p * 16);
    }
    c.srdT = mk_srd(ws + WS_WOT, DQ * D * 2); c.sofT2 = ((head & 7) * 128 + (head >> 3) * 32) * 5120 + ((bid >> 6) * 40 + w * 5) * 1024;
    c.m0S = sb + L2_SIDE + w * 4096;
    c.pS = (const LAS char*)(size_t)(c.m0S + g * 1024 + q * 64 + p * 8);
  }
  bf16x8 hf[10][4];
  {
    asm volatile("s_waitcnt vmcnt(6)" : "+v"(m0_), "+v"(m1_), "+v"(m2_), "+v"(m3_), "+v"(ln), "+v"(xv[0]), "+v"(xv[1]) :: "memory");
    const f32x2 s0 = __builtin_bit_cast(f32x2, m0_) + __builtin_bit_cast(f32x2, m2_), c0 = __builtin_bit_cast(f32x2, m1_) + __builtin_bit_cast(f32x2, m3_);
    LAS char* ab = (LAS char*)(size_t)c.m0S;
    *(LAS f32x2*)(ab + min(lane, 39) * 8) = (f32x2){rbf(1.f + rbf(c0[0])), rbf(1.f + rbf(c0[1]))};
    *(LAS f32x2*)(ab + 320 + min(lane, 39) * 8) = (f32x2){rbf(s0[0]) * QSCALE, rbf(s0[1]) * QSCALE};
    const float mean = __uint_as_float(ln[0]), rs = __uint_as_float(ln[1]) * QSCALE;
#pragma unroll
    for (int it = 0; it < 2; ++it) {
      const int ch = it ? 8 + (hch & 1) : hch;
      const f32x4 a0 = *(const LAS f32x4*)(ab + ch * 32), a1 = *(const LAS f32x4*)(ab + ch * 32 + 16), b0 = *(const LAS f32x4*)(ab + 320 + ch * 32), b1 = *(const LAS f32x4*)(ab + 336 + ch * 32);
      const u32x4 xx = __builtin_bit_cast(u32x4, xv[it]);
      u32x4 o;
      o[0] = pk2((bflo(xx[0]) - mean) * rs * a0[0] + b0[0], (bfhi(xx[0]) - mean) * rs * a0[1] + b0[1]);
      o[1] = pk2((bflo(xx[1]) - mean) * rs * a0[2] + b0[2], (bfhi(xx[1]) - mean) * rs * a0[3] + b0[3]);
      o[2] = pk2((bflo(xx[2]) - mean) * rs * a1[0] + b1[0], (bfhi(xx[2]) - mean) * rs * a1[1] + b1[1]);
      o[3] = pk2((bflo(xx[3]) - mean) * rs * a1[2] + b1[2], (bfhi(xx[3]) - mean) * rs * a1[3] + b1[3]);
      st16<16>(nopoison(o), c.srdH, it ? ho1 : ho0);
    }
  }
#pragma unroll
  for (int dt = 0; dt < 16; ++dt) { c.o[dt >> 1][dt & 1] = f32x4{}; PIN(c.o[dt >> 1][dt & 1]); }
  STAMP(1);
  asm volatile("s_barrier" ::: "memory");
  first8(hf[0], hf[1], c.voffH, c.srdH, c.sofH, c.sofH + 4096);
  asm volatile("s_barrier" ::: "memory");
  STAMP(2);
  frag_ld_all<0>(c, std::make_integer_sequence<int, 8>{}); frag_ld_all<1>(c, std::make_integer_sequence<int, 8>{});
  SBAR();
  k2_quads<0>(c, hf, std::make_integer_sequence<int, 20>{});
  STAMP(3);
  k2_quads<20>(c, hf, std::make_integer_sequence<int, 20>{});
  asm volatile("s_nop 7" ::: "memory"); SBAR();
  STAMP(4);
  q_pack_last(c, std::make_integer_sequence<int, 8>{});
  c.negm[0] = c.negm[1] = f32x4{};
  SBAR();
  asm volatile("s_nop 1" : "+v"(c.negm[0]), "+v"(c.negm[1]), "+v"(c.qf[0][0]), "+v"(c.qf[3][1]));
  k2_quad<40>(c, hf); k2_quad<41>(c, hf);
  asm volatile("s_nop 7" ::: "memory"); SBAR();
  c.aone = lane < 16 ? 1.f : 0.f;
#pragma unroll
  for (int u = 0; u < 2; ++u) {
    const f32x4 a = c.sA[0][u], b = c.sA[1][u];
    const float m = max1632(fmaxf(fmaxf(fmaxf(a[0], a[1]), fmaxf(a[2], a[3])), fmaxf(fmaxf(b[0], b[1]), fmaxf(b[2], b[3]))));
    c.sA[0][u] -= m; c.sA[1][u] -= m; c.negm[u] = (f32x4){-m, -m, -m, -m}; PIN(c.negm[u]); c.fp[u] = 1.f;
#pragma unroll
    for (int r = 0; r < 4; ++r) { c.sA[0][u][r] = EX2(c.sA[0][u][r]); if (u == 0) c.sA[1][0][r] = EX2(c.sA[1][0][r]); }
    c.l[u] = (c.sA[0][u][0] + c.sA[0][u][1]) + (c.sA[0][u][2] + c.sA[0][u][3]);
  }
  c.l[0] += c.sA[1][0][0] + c.sA[1][0][1];
  SBAR();
  STAMP(5);
  k2_quads<42>(c, hf, std::make_integer_sequence<int, 8>{});
  STAMP(6);
  k2_quads<50>(c, hf, std::make_integer_sequence<int, 24>{});
  STAMP(7);
  k2_quads<74>(c, hf, std::make_integer_sequence<int, 30>{});
  asm volatile("s_nop 7" ::: "memory"); SBAR();
  STAMP(8);
  char* ob = ws + WS_O + (size_t)((qb * 8 + w) * 128 + head * 4) * 2048 + lane * 16;
#pragma unroll
  for (int u = 0; u < 2; ++u) {
    const float il = 1.f / sum1632(c.l[u]);
#pragma unroll
    for (int s = 0; s < 4; ++s) *(bf16x8*)(ob + s * 2048 + u * 1024) = pack44(c.o[2 * s][u], c.o[2 * s + 1][u], il);
  }
  STAMP(9); STAMP_FLUSH(2, STAMP_WAVE);
}

#define W4A 2
typedef __attribute__((address_space(1))) unsigned gu32;
constexpr int RLD = 84;
constexpr int L_OWN = 131072, L_CS = L_OWN, L_W5X = L_OWN + 1024, L_MOD = L_OWN + 30720, LDS3 = L_MOD + 2048;
struct K3C {
  __amdgpu_buffer_rsrc_t srdW, srdO, srdM, srdX; unsigned voff, m0base, sofW, sofO, voffM, voffX, voffX1;
  const LAS char *pLo, *pHi; bf16x8 fw[3][5], xv[2]; f32x4 acc[2][5]; float m0_, m1_;
};
constexpr int k3_wr(int j) { return j < 16 ? 1 : 0; }
constexpr int k3_r0(int j) { return 4 + j; }
constexpr bool k3_bar(int j) { return j == 1 || j == 4 || j == 6 || j == 9 || j == 14 || j == 20; }
constexpr int k3_ex(int j) { return j == 28 ? 4 : 0; }
constexpr int k3_ops(int j) { return k3_wr(j) + k3_ex(j) + (j + 4 < 32 ? 2 : 0); }
constexpr int k3_wait(int j) { return j == 0 ? 8 : j == 1 ? 4 + k3_ops(0) : j == 2 ? 2 + k3_ops(0) + k3_ops(1) : k3_ops(j - 3) + k3_ops(j - 2) + k3_ops(j - 1); }
template <int R> DI void k3_dw(const K3C& c) { dma16i<(R & 15) * 8192, R * 8192>(c.m0base, c.voff, c.srdW, c.sofW); }
template <int J, int R2> DI void k3_lo(const K3C& c, bf16x8& r) { ld16<R2 * 1024 + (J & 1) * 2048>(r, c.voff, c.srdO, c.sofO + (J >> 1) * 4096); }
DI bf16x8 k3_rd(const K3C& c, int piece) { const int pp = piece & 127; return lds_rd(pp < 64 ? c.pLo + pp * 1024 : c.pHi + (pp - 64) * 1024); }
DI float xor8(float v) { return __int_as_float(__builtin_amdgcn_update_dpp(0, __float_as_int(v), 0x128, 0xf, 0xf, true)); }
DI float sum816(float v) { return sum1632(v + xor8(v)); }
DI void cnt_add(char* ws, int i) { __hip_atomic_fetch_add((gu32*)(ws + WS_CNT + i * 128), 1u, __ATOMIC_RELAXED, __HIP_MEMORY_SCOPE_AGENT); }
DI void cnt_wait(char* ws, int i, unsigned n) {
  gu32* cnt = (gu32*)(ws + WS_CNT + i * 128);
  for (unsigned spins = 0; rfl(__hip_atomic_load(cnt, __ATOMIC_RELAXED, __HIP_MEMORY_SCOPE_AGENT)) < n && spins < (1u << 22); ++spins) __builtin_amdgcn_s_sleep(1);
  asm volatile("" ::: "memory");
}
template <int J, int M> DI void k3_fill(K3C& c, bf16x8 (&of)[32][2]) {
  if constexpr ((M & 1) == 0) { if constexpr (J + 2 < 32) c.fw[(J + 2) % 3][M >> 1] = k3_rd(c, 5 * (J + 2) + (M >> 1)); }
  else if constexpr (M == 1) {
    if constexpr (k3_wr(J) >= 1) k3_dw<k3_r0(J)>(c);
    if constexpr (J == 28) asm volatile("buffer_load_dword %0, %2, %3, 0 offen\n\tbuffer_load_dword %1, %2, %3, %4 offen" : "=&v"(c.m0_), "=&v"(c.m1_) : "v"(c.voffM), "s"(c.srdM), "s"(6 * D * 4) : "memory");
  } else if constexpr (M == 3) { if constexpr (k3_wr(J) == 2) k3_dw<k3_r0(J) + 1>(c); }
  else if constexpr (M == 5) { if constexpr (J + 4 < 32) k3_lo<J + 4, 0>(c, of[J + 4][0]); if constexpr (J == 28) ld16<0>(c.xv[0], c.voffX, c.srdX, 0); }
  else if constexpr (M == 7) { if constexpr (J + 4 < 32) k3_lo<J + 4, 1>(c, of[J + 4][1]); if constexpr (J == 28) ld16<0>(c.xv[1], c.voffX1, c.srdX, 0); }
}
template <int J, int M> DI void k3_slot(K3C& c, bf16x8 (&of)[32][2]) { MFMA16_ACC(c.acc[M & 1][M >> 1], c.fw[J % 3][M >> 1], of[J][M & 1]); k3_fill<J, M>(c, of); SBAR(); }
template <int J> DI void k3_step(K3C& c, bf16x8 (&of)[32][2]) {
  bf16x8 (&f)[5] = c.fw[J % 3];
  if constexpr (k3_bar(J)) asm volatile("s_waitcnt vmcnt(%7)\n\ts_barrier" : "+v"(of[J][0]), "+v"(of[J][1]), "+v"(f[0]), "+v"(f[1]), "+v"(f[2]), "+v"(f[3]), "+v"(f[4]) : "i"(k3_wait(J)) : "memory");
  else asm volatile("s_waitcnt vmcnt(%7)" : "+v"(of[J][0]), "+v"(of[J][1]), "+v"(f[0]), "+v"(f[1]), "+v"(f[2]), "+v"(f[3]), "+v"(f[4]) : "i"(k3_wait(J)) : "memory");
  k3_slot<J, 0>(c, of); k3_slot<J, 1>(c, of); k3_slot<J, 2>(c, of); k3_slot<J, 3>(c, of); k3_slot<J, 4>(c, of);
  k3_slot<J, 5>(c, of); k3_slot<J, 6>(c, of); k3_slot<J, 7>(c, of); k3_slot<J, 8>(c, of); k3_slot<J, 9>(c, of);
}
template <int... Js> DI void k3_steps(K3C& c, bf16x8 (&of)[32][2], std::integer_sequence<int, Js...>) { (k3_step<Js>(c, of), ...); }

struct K4C {
  __amdgpu_buffer_rsrc_t srdG, srdH, srdS, srdD; unsigned voffH, m0base, sofW, sofH, aW, voffSt, sofcs, sofcnt, m0x, flag, rot[7];
  const LAS char* pW; bf16x8 f[3][4], st[2], cs4, wr[20]; f32x4 o[8][2]; LAS char* csimg; char* ws; int j;
};
template <int I, int HALF> DI void k4_dma1(const K4C& c) { dma16i<((I + 2) & (NSLOT - 1)) * SLOT + HALF * 1024, I * 16384 + HALF * 1024>(c.m0base, c.voffH, c.srdG, c.sofW); }
template <int I> DI void k4_dma(const K4C& c) { k4_dma1<I, 0>(c); k4_dma1<I, 1>(c); }
template <int I, int S_> DI void k4_ldh1(const K4C& c, bf16x8& r) { ld16c<S_ * 1024>(r, c.voffH, c.srdH, c.sofH + I * 4096); }
template <int I> DI void k4_ldh(const K4C& c, bf16x8 (&hf)[4]) { k4_ldh1<I, 0>(c, hf[0]); k4_ldh1<I, 1>(c, hf[1]); k4_ldh1<I, 2>(c, hf[2]); k4_ldh1<I, 3>(c, hf[3]); }
template <int Q, int E> DI void k4_rd(K4C& c) {
  if constexpr (Q < 40) { if constexpr ((Q & 3) == 0 && E == 0) c.pW = slot_ptr(c.aW, (Q >> 2) + 2); c.f[Q % 3][E] = lds_rd(c.pW + ((Q & 3) * 4 + E) * 1024); }
}
constexpr int w5_off(int p) { return ((4 + (p >> 1)) & 7) * SLOT + (p & 1) * 1024; }
template <int P> DI void w5_dma(const K4C& c) { dma16(P < 14 ? c.m0base + w5_off(P) : c.m0x, c.voffH, c.srdD, c.rot[4 + P / 5] * 5120 + (P % 5) * 1024); }
template <int P> DI void w5_ld(K4C& c) { ld16<(P % 5 & 3) * 1024>(c.wr[P], c.voffH, c.srdD, c.rot[P / 5] * 5120 + (P % 5 >> 2) * 4096); }
constexpr int k4_wait(int i) { return i == 0 ? 4 : i == 1 ? 8 : i == 2 ? 10 : i < 7 ? 12 : i == 7 ? 10 : 13; }
template <int Q, int M> DI void k4_slot(K4C& c, bf16x8 (&hf)[10][4]) {
  constexpr int I = Q >> 2, X = (Q & 3) * 8 + M, dt = 2 * (Q & 3) + (M >> 2), u = (M >> 1) & 1, ks = M & 1;
  if constexpr (I == 0 && ks == 0) asm volatile("v_mfma_f32_16x16x32_bf16 %0, %1, %2, 0" : "=&v"(c.o[dt][u]) : "v"(c.f[Q % 3][2 * (M >> 2)]), "v"(hf[0][u]));
  else MFMA16_ACC(c.o[dt][u], c.f[Q % 3][2 * (M >> 2) + ks], hf[I][2 * ks + u]);
  SBAR();
  if constexpr ((M & 1) == 0) k4_rd<Q + 2, (M >> 1)>(c);
  if constexpr (I == 0) { if constexpr ((X & 3) == 1) k4_ldh1<2 + (X >> 4), (X >> 2) & 3>(c, hf[2 + (X >> 4)][(X >> 2) & 3]); }
  else {
    if constexpr (X < 16 && (X & 3) == 1 && I + 3 < 10) k4_ldh1<I + 3, (X >> 2)>(c, hf[I + 3][X >> 2]);
    if constexpr (X == 19 || X == 27) { if constexpr (I < 3) k4_dma1<I + 7, (X == 27)>(c); else w5_dma<2 * (I - 3) + (X == 27)>(c); }
  }
  if constexpr (I == 8 && X == 1) asm volatile("buffer_load_dword %0, off, %1, %2 sc1" : "=v"(c.flag) : "s"(c.srdS), "s"(c.sofcnt) : "memory");
  if constexpr (I == 9 && X == 1) {
    asm volatile("s_waitcnt vmcnt(12)" : "+v"(c.flag) :: "memory");
    if (__builtin_expect(rfl(c.flag) == 0u, 0)) cnt_wait(c.ws, CNT_C + c.j, 1u);
    ld16c<0>(c.cs4, c.voffH, c.srdS, c.sofcs);
  }
  if constexpr (I == 7 && (X == 3 || X == 7)) ld16c<(X == 7) * 1024>(c.st[X == 7], c.voffSt, c.srdS, WS_STAT);
  if constexpr ((I == 7 || I == 8) && (X == 17 || X == 21 || X == 25 || X == 29)) w5_ld<10 * (I - 7) + ((X - 17) >> 2)>(c);
  if constexpr ((I == 8 || I == 9) && X >= 3 && X < 15 && (X & 1)) w5_ld<10 * (I - 8) + 4 + ((X - 3) >> 1)>(c);
  if constexpr (I == 9 && X == 31) w5_dma<14>(c);
  SBAR();
}
template <int Q> DI void k4_quad(K4C& c, bf16x8 (&hf)[10][4]) {
  constexpr int I = Q >> 2;
  if constexpr ((Q & 3) == 2) {
    if constexpr (I < 9) wait_bar4v<k4_wait(I)>(hf[I + 1][0], hf[I + 1][1], hf[I + 1][2], hf[I + 1][3]); else asm volatile("s_barrier" ::: "memory");
    if constexpr (I == 8) asm volatile("" : "+v"(c.st[0]), "+v"(c.st[1]));
  }
  __builtin_amdgcn_s_setprio(3 - ((Q + 2) & 3));
  asm volatile("" :: "v"(c.f[Q % 3][0]), "v"(c.f[Q % 3][1]), "v"(c.f[Q % 3][2]), "v"(c.f[Q % 3][3]));
  SBAR();
  k4_slot<Q, 0>(c, hf); k4_slot<Q, 1>(c, hf); k4_slot<Q, 2>(c, hf); k4_slot<Q, 3>(c, hf); k4_slot<Q, 4>(c, hf); k4_slot<Q, 5>(c, hf); k4_slot<Q, 6>(c, hf); k4_slot<Q, 7>(c, hf);
}
template <int... Qs> DI void k4_quads(K4C& c, bf16x8 (&hf)[10][4], std::integer_sequence<int, Qs...>) { (k4_quad<Qs>(c, hf), ...); }
#define WR_ALL(c) "+v"(c.wr[0]), "+v"(c.wr[1]), "+v"(c.wr[2]), "+v"(c.wr[3]), "+v"(c.wr[4]), "+v"(c.wr[5]), "+v"(c.wr[6]), "+v"(c.wr[7]), "+v"(c.wr[8]), "+v"(c.wr[9]), \
                  "+v"(c.wr[10]), "+v"(c.wr[11]), "+v"(c.wr[12]), "+v"(c.wr[13]), "+v"(c.wr[14]), "+v"(c.wr[15]), "+v"(c.wr[16]), "+v"(c.wr[17]), "+v"(c.wr[18]), "+v"(c.wr[19])
template <int... Ps> DI void w5_dmas(const K4C& c, std::integer_sequence<int, Ps...>) { (w5_dma<Ps>(c), ...); }
template <int... Ps> DI void w5_lds(K4C& c, std::integer_sequence<int, Ps...>) { (w5_ld<Ps>(c), ...); }
DI void k5_la(bf16x8 (&r)[4], unsigned voff, __amdgpu_buffer_rsrc_t srd, unsigned so) {
  ld16c<0>(r[0], voff, srd, so); ld16c<1024>(r[1], voff, srd, so); ld16c<2048>(r[2], voff, srd, so); ld16c<3072>(r[3], voff, srd, so);
}

__global__ __launch_bounds__(512, 2) void k3_tail(const u16* __restrict__ wg, const u16* __restrict__ wu, char* __restrict__ ws, u16* __restrict__ out) {
  extern __shared__ __attribute__((aligned(16))) char smem[];
  LAS char* sm = (LAS char*)smem;
  const unsigned sb = (unsigned)(size_t)sm;
  const int tid = threadIdx.x, lane = tid & 63, bid = blockIdx.x;
  const unsigned w = rfl(tid >> 6);
  const int kq = (bid & 7) >> 1, rb = (bid & 1) * 4 + (bid >> 6), nt = (bid >> 3) & 7;
  STAMP_DECL; STAMP(0);
  const int frow = rb * 256 + kq * 64 + w * 8 + (lane & 7), fch = lane >> 3;
  const int g4 = lane >> 4, q16 = lane & 15;
  const int j4 = kq * 7 + nt; const bool act4 = nt < 7 && j4 < 27;
  u32x4 x1[2];
  K4C d;
  {
    K3C c;
    c.srdW = mk_srd(ws + WS_WOT, DQ * D * 2); c.srdO = mk_srd(ws + WS_O, S * DQ * 2);
    c.voff = lane * 16; c.m0base = sb + w * 1024;
    c.sofW = (nt * 128 + kq * 32) * 5120 + w * 1024; c.sofO = ((rb * 8 + w) * 128 + kq * 32) * 2048;
    asm volatile("s_nop 4" ::: "memory");
    bf16x8 of[32][2];
    k3_dw<0>(c); k3_dw<1>(c); k3_lo<0, 0>(c, of[0][0]); k3_lo<0, 1>(c, of[0][1]);
    asm volatile("s_barrier" ::: "memory");
    k3_dw<2>(c); k3_dw<3>(c); k3_lo<1, 0>(c, of[1][0]); k3_lo<1, 1>(c, of[1][1]);
    asm volatile("s_barrier" ::: "memory");
    k3_lo<2, 0>(c, of[2][0]); k3_lo<2, 1>(c, of[2][1]); k3_lo<3, 0>(c, of[3][0]); k3_lo<3, 1>(c, of[3][1]);
    c.pLo = sm + lane * 16; { unsigned a = sb + lane * 16 + 65536; PIN(a); c.pHi = (const LAS char*)(size_t)a; }
    c.srdM = mk_srd(ws + WS_MOD, 2 * 6 * D * 4); c.srdX = mk_srd(ws + WS_XT, S * D * 2);
    { const int t = min(tid, 239); c.voffM = 4 * (nt * 80 + (t < 80 ? 2 * D + t : t < 160 ? 4 * D + t - 80 : 5 * D + t - 160)); }
    c.voffX = h_off(frow, nt * 10 + fch); c.voffX1 = h_off(frow, nt * 10 + 8 + (fch & 1));
#pragma unroll
    for (int i = 0; i < 10; ++i) { c.acc[i / 5][i % 5] = f32x4{}; PIN(c.acc[i / 5][i % 5]); }
    asm volatile("s_waitcnt vmcnt(%2)\n\ts_barrier" : "+v"(of[0][0]), "+v"(of[0][1]) : "i"(k3_wait(0)) : "memory");
    STAMP(1);
#pragma unroll
    for (int cc = 0; cc < 10; ++cc) c.fw[cc / 5][cc % 5] = k3_rd(c, cc);
    SBAR();
    k3_steps(c, of, std::make_integer_sequence<int, 32>{});
    asm volatile("s_nop 7" ::: "memory"); SBAR();
    STAMP(2);
    const int tile = rb * 8 + nt, own = w >> 1;
    d.srdG = mk_srd(ws + WS_WGU, act4 ? D * F * 4 : 0);
    d.voffH = lane * 16; d.m0base = sb + w * 2048; d.sofW = j4 * 163840 + w * 2048;
    const __amdgpu_buffer_rsrc_t srdP = mk_srd(ws + WS_PART, 64 * 4 * 3 * 10240);
    if (own != kq) {
      const unsigned so = ((tile * 4 + own) * 3 + (kq - (kq > own))) * 10240 + (w & 1) * 5120;
      const unsigned vo = ((g4 & 1) * 2 + (q16 >> 3)) * 1280 + (g4 >> 1) * 128 + (q16 & 7) * 16;
#pragma unroll
      for (int cc = 0; cc < 5; ++cc) {
        const auto r0 = __builtin_amdgcn_permlane16_swap(pk2(c.acc[0][cc][0], c.acc[0][cc][1]), pk2(c.acc[1][cc][0], c.acc[1][cc][1]), false, false);
        const auto r1 = __builtin_amdgcn_permlane16_swap(pk2(c.acc[0][cc][2], c.acc[0][cc][3]), pk2(c.acc[1][cc][2], c.acc[1][cc][3]), false, false);
        st16<16>((u32x4){r0[0], r1[0], r0[1], r1[1]}, srdP, vo + so + cc * 256);
      }
      asm volatile("s_waitcnt vmcnt(0)" ::: "memory");
      if (lane == 0) cnt_add(ws, CNT_P + tile * 4 + own);
    } else {
#pragma unroll
      for (int cc = 0; cc < 5; ++cc)
#pragma unroll
        for (int r2 = 0; r2 < 2; ++r2) *(LAS f32x4*)(sm + L_OWN + (((w & 1) * 32 + r2 * 16 + q16) * RLD + cc * 16 + g4 * 4) * 4) = c.acc[r2][cc];
      asm volatile("s_waitcnt vmcnt(0)" ::: "memory");
    }
    k4_dma<0>(d);
    k4_dma<1>(d);
    asm volatile("" : "+v"(c.m0_), "+v"(c.m1_), "+v"(c.xv[0]), "+v"(c.xv[1]));
    { const float mv = rbf(c.m0_ + c.m1_); *(LAS float*)(sm + L_MOD + tid * 4) = (tid >= 80 && tid < 160) ? rbf(1.f + mv) : mv; }
    STAMP(3);
    if (w == 2 * kq) cnt_wait(ws, CNT_P + tile * 4 + kq, 6u);
    asm volatile("s_waitcnt lgkmcnt(0)\n\ts_barrier" ::: "memory");
    STAMP(4);
    u32x4 pv[2][3];
#pragma unroll
    for (int sI = 0; sI < 3; ++sI) {
      const unsigned so = ((tile * 4 + kq) * 3 + sI) * 10240 + w * 1280;
      pv[0][sI] = __builtin_amdgcn_raw_buffer_load_b128(srdP, c.voff, so, 16);
      pv[1][sI] = __builtin_amdgcn_raw_buffer_load_b128(srdP, 1024 + (lane & 15) * 16, so, 16);
    }
    const __amdgpu_buffer_rsrc_t srdY = mk_srd(ws + WS_Y, S * D * 2);
    float v[2][8];
#pragma unroll
    for (int it = 0; it < 2; ++it) {
      const int ch = it ? 8 + (fch & 1) : fch;
      const LAS float* po = (const LAS float*)(sm + L_OWN) + (w * 8 + (lane & 7)) * RLD + ch * 8;
      const LAS float* pm = (const LAS float*)(sm + L_MOD) + ch * 8;
      f32x4 t[2] = {*(const LAS f32x4*)po, *(const LAS f32x4*)(po + 4)};
      const f32x4 ga[2] = {*(const LAS f32x4*)pm, *(const LAS f32x4*)(pm + 4)}, a1[2] = {*(const LAS f32x4*)(pm + 80), *(const LAS f32x4*)(pm + 84)};
      const u32x4 xx = __builtin_bit_cast(u32x4, c.xv[it]);
      u32x4 y;
#pragma unroll
      for (int e = 0; e < 4; ++e) {
        const float lo = t[e >> 1][2 * (e & 1)] + ((bflo(pv[it][0][e]) + bflo(pv[it][1][e])) + bflo(pv[it][2][e]));
        const float hi = t[e >> 1][2 * (e & 1) + 1] + ((bfhi(pv[it][0][e]) + bfhi(pv[it][1][e])) + bfhi(pv[it][2][e]));
        x1[it][e] = pk2(bflo(xx[e]) + ga[e >> 1][2 * (e & 1)] * lo, bfhi(xx[e]) + ga[e >> 1][2 * (e & 1) + 1] * hi);
        v[it][2 * e] = bflo(x1[it][e]); v[it][2 * e + 1] = bfhi(x1[it][e]);
        y[e] = pk2(v[it][2 * e] * a1[e >> 1][2 * (e & 1)], v[it][2 * e + 1] * a1[e >> 1][2 * (e & 1) + 1]);
      }
      st16<16>(y, srdY, it == 0 ? c.voffX : lane < 16 ? c.voffX1 : 0x7fffffffu);
    }
    {
      float s0 = 0.f, s1 = 0.f;
#pragma unroll
      for (int e = 0; e < 8; ++e) { s0 += v[0][e]; s1 += v[1][e]; }
      const float mu = sum816(lane < 16 ? s0 + s1 : s0) * (1.f / 80.f);
      float q0 = 0.f, q1 = 0.f;
#pragma unroll
      for (int e = 0; e < 8; ++e) { q0 += (v[0][e] - mu) * (v[0][e] - mu); q1 += (v[1][e] - mu) * (v[1][e] - mu); }
      const float m2 = sum816(lane < 16 ? q0 + q1 : q0);
      d.srdS = mk_srd(ws, WS_H);
      __builtin_amdgcn_raw_buffer_store_b64((u32x2){__float_as_uint(mu), __float_as_uint(m2)}, d.srdS, lane < 8 ? frow * 64 + nt * 8 : 0x7fffffffu, WS_STAT, 16);
    }
    STAMP(5);
    asm volatile("s_waitcnt vmcnt(0)\n\ts_barrier" ::: "memory");
    if (tid < 32) cnt_add(ws, CNT_Y + rb * 32 + tid);
    STAMP(6);
  }
  {
    const int k0 = w < 6 ? 7 * w : 42 + 6 * (w - 6), nk = w < 6 ? 7 : 6;
    d.srdD = mk_srd(ws + WS_WDT + (size_t)(nt * 54 + k0) * 5120, nk * 5120);
    d.m0x = sb + L_W5X + w * 4096;
#pragma unroll
    for (int i = 0; i < 7; ++i) { const int r = i + (nt == 7 ? 3 : nt); d.rot[i] = r >= 7 ? r - 7 : r; }
  }
  if (act4) {
    k4_dma<2>(d); k4_dma<3>(d); k4_dma<4>(d); k4_dma<5>(d); k4_dma<6>(d); k4_dma<7>(d);
    d.srdH = mk_srd(ws + WS_Y, S * D * 2); d.sofH = (rb * 8 + w) * 40 * 1024;
    d.aW = sb + lane * 16;
    d.sofcs = WS_CSBS + j4 * 1024; d.sofcnt = WS_CNT + (CNT_C + j4) * 128; d.voffSt = (rb * 256 + w * 32 + q16) * 64 + g4 * 16; d.csimg = sm + L_CS + w * 4096 + lane * 16; d.ws = ws; d.j = j4;
    if (w == 0) cnt_wait(ws, CNT_Y + rb * 32 + kq * 8 + nt, 32u);
    asm volatile("s_barrier" ::: "memory");
    STAMP(7);
    bf16x8 hf[10][4];
    k4_ldh<0>(d, hf[0]); k4_ldh<1>(d, hf[1]);
    wait_bar4v<4>(hf[0][0], hf[0][1], hf[0][2], hf[0][3]);
    STAMP(8);
    k4_rd<0, 0>(d); k4_rd<0, 1>(d); k4_rd<0, 2>(d); k4_rd<0, 3>(d); k4_rd<1, 0>(d); k4_rd<1, 1>(d); k4_rd<1, 2>(d); k4_rd<1, 3>(d);
    SBAR();
    k4_quads(d, hf, std::make_integer_sequence<int, 40>{});
    __builtin_amdgcn_s_setprio(0);
    STAMP(9);
    asm volatile("s_waitcnt vmcnt(9)" : "+v"(d.cs4) :: "memory"); *(LAS bf16x8*)d.csimg = d.cs4;
    float nmr[2], rg[2], ru[2];
#pragma unroll
    for (int u = 0; u < 2; ++u) {
      const f32x4 sv = __builtin_bit_cast(f32x4, d.st[u]);
      const float mean = sum1632(sv[0] + sv[2]) * 0.125f;
      const float m2 = sum1632(sv[1] + sv[3] + 80.f * ((sv[0] - mean) * (sv[0] - mean) + (sv[2] - mean) * (sv[2] - mean)));
      const float rs = rsqrtf(m2 * (1.f / D) + EPS);
      nmr[u] = -mean * rs; rg[u] = rs * -1.4426950408889634f; ru[u] = rs * -0.6931471805599453f;
    }
    asm volatile("s_nop 3" : "+v"(nmr[0]), "+v"(nmr[1])); SBAR();
    const LAS float* cb = (const LAS float*)(sm + L_CS + w * 4096) + g4 * 8;
    const __amdgpu_buffer_rsrc_t srdA = mk_srd(ws + WS_ACT, S * F * 2);
    const unsigned vo = (((rb * 4 + (w >> 1)) * 54 + j4 * 2) * 4 + (w & 1) * 2) * 1024 + lane * 16;
#pragma unroll
    for (int s = 0; s < 2; ++s) {
      u32x4 o[2];
#pragma unroll
      for (int t = 0; t < 2; ++t) {
        const f32x4 csg = *(const LAS f32x4*)(cb + 32 * s + 4 * t), bsg = *(const LAS f32x4*)(cb + 64 + 32 * s + 4 * t);
        const f32x4 csu = *(const LAS f32x4*)(cb + 128 + 32 * s + 4 * t), bsu = *(const LAS f32x4*)(cb + 192 + 32 * s + 4 * t);
#pragma unroll
        for (int u = 0; u < 2; ++u)
#pragma unroll
          for (int e = 0; e < 2; ++e) {
            const f32x2 ag = {d.o[4 * s + t][u][2 * e], d.o[4 * s + t][u][2 * e + 1]}, au = {d.o[4 * s + 2 + t][u][2 * e], d.o[4 * s + 2 + t][u][2 * e + 1]};
            const f32x2 g = ag * rg[u] + ((f32x2){csg[2 * e], csg[2 * e + 1]} * nmr[u] + (f32x2){bsg[2 * e], bsg[2 * e + 1]});
            const f32x2 uu = au * ru[u] + ((f32x2){csu[2 * e], csu[2 * e + 1]} * nmr[u] + (f32x2){bsu[2 * e], bsu[2 * e + 1]});
            const f32x2 den = (f32x2){EX2(g[0]), EX2(g[1])} + 1.f;
            const f32x2 a = (g * uu) * (f32x2){__builtin_amdgcn_rcpf(den[0]), __builtin_amdgcn_rcpf(den[1])};
            o[u][2 * t + e] = pk2(a[0], a[1]);
          }
      }
      st16<16>(o[0], srdA, vo + s * 4096); st16<16>(o[1], srdA, vo + s * 4096 + 1024);
    }
    STAMP(10);
    asm volatile("s_waitcnt vmcnt(0)\n\ts_barrier" : WR_ALL(d) :: "memory");
    if (tid < 32) cnt_add(ws, CNT_A + rb * 32 + tid);
  } else {
    const int jc = (bid >> 6) * 8 + (bid & 7);
    asm volatile("s_waitcnt vmcnt(0)\n\ts_barrier" ::: "memory");
    if (nt == 7 && jc < 27) {
      const float* mod = (const float*)(ws + WS_MOD);
      const int ch = tid & 15, rg = tid >> 4;
      float mv[3];
#pragma unroll
      for (int i = 0; i < 3; ++i) { const int t = min(tid + 512 * i, 1279); mv[i] = mod[3 * D + t] + mod[9 * D + t]; }
      const u16* wsrc = ((ch & 8) ? wu : wg) + jc * 64 + (ch & 7) * 8;
      u32x4 wv[20];
#pragma unroll
      for (int i = 0; i < 20; ++i) wv[i] = *(const u32x4*)(wsrc + (size_t)(rg + 32 * i) * F);
      LAS float* mm = (LAS float*)sm;
#pragma unroll
      for (int i = 0; i < 3; ++i) { const int t = min(tid + 512 * i, 1279); const float r = rbf(mv[i]); mm[t] = t < D ? r : rbf(1.f + r); }
      asm volatile("s_waitcnt lgkmcnt(0)\n\ts_barrier" ::: "memory");
      float ac[8] = {0, 0, 0, 0, 0, 0, 0, 0}, ab[8] = {0, 0, 0, 0, 0, 0, 0, 0};
#pragma unroll
      for (int i = 0; i < 20; ++i) {
        const float sbv = mm[rg + 32 * i], a = mm[D + rg + 32 * i];
#pragma unroll
        for (int e = 0; e < 4; ++e) { const float lo = bflo(wv[i][e]), hi = bfhi(wv[i][e]); ac[2 * e] += a * lo; ac[2 * e + 1] += a * hi; ab[2 * e] += sbv * lo; ab[2 * e + 1] += sbv * hi; }
      }
#pragma unroll
      for (int e = 0; e < 8; ++e) { ac[e] = sum1632(ac[e]); ab[e] = sum1632(ab[e]); }
      LAS float* rd = mm + 2048 + w * 256 + (ch >> 3) * 128 + (ch & 7) * 8;
      if (lane < 16) { *(LAS f32x4*)rd = (f32x4){ac[0], ac[1], ac[2], ac[3]}; *(LAS f32x4*)(rd + 4) = (f32x4){ac[4], ac[5], ac[6], ac[7]};
                       *(LAS f32x4*)(rd + 64) = (f32x4){ab[0], ab[1], ab[2], ab[3]}; *(LAS f32x4*)(rd + 68) = (f32x4){ab[4], ab[5], ab[6], ab[7]}; }
      asm volatile("s_waitcnt lgkmcnt(0)\n\ts_barrier" ::: "memory");
      if (tid < 256) {
        const LAS float* r = mm + 2048 + tid;
        const float t = (((r[0] + r[256]) + (r[512] + r[768])) + ((r[1024] + r[1280]) + (r[1536] + r[1792]))) * (tid < 128 ? -1.4426950408889634f : -0.6931471805599453f);
        __builtin_amdgcn_raw_buffer_store_b32(__float_as_uint(t), d.srdS, tid * 4, WS_CSBS + jc * 1024, 16);
      }
      asm volatile("s_waitcnt vmcnt(0) lgkmcnt(0)\n\ts_barrier" ::: "memory");
      if (tid == 0) cnt_add(ws, CNT_C + jc);
    }
    w5_dmas(d, std::make_integer_sequence<int, 15>{}); w5_lds(d, std::make_integer_sequence<int, 20>{});
    asm volatile("s_waitcnt vmcnt(0)" : WR_ALL(d) :: "memory");
  }
  STAMP(11);
  {
    const int k0 = w < 6 ? 7 * w : 42 + 6 * (w - 6), nk = w < 6 ? 7 : 6;
    const __amdgpu_buffer_rsrc_t srdA = mk_srd(ws + WS_ACT + (size_t)((rb * 4 + kq) * 54 + k0) * 4096, nk * 4096);
    f32x4 gb[2][2];
#pragma unroll
    for (int it = 0; it < 2; ++it) { const LAS float* pm = (const LAS float*)(sm + L_MOD) + 160 + (it ? 8 + (fch & 1) : fch) * 8; gb[it][0] = *(const LAS f32x4*)pm; gb[it][1] = *(const LAS f32x4*)(pm + 4); }
    if (w == 0) cnt_wait(ws, CNT_A + rb * 32 + kq * 8 + nt, 27u);
    asm volatile("s_waitcnt lgkmcnt(0)\n\ts_barrier\n\ts_nop 4" : "+v"(gb[0][0]), "+v"(gb[0][1]), "+v"(gb[1][0]), "+v"(gb[1][1]) :: "memory");
    STAMP(12);
    bf16x8 fa[7][4];
    k5_la(fa[0], d.voffH, srdA, d.rot[0] * 4096);
    asm volatile("s_barrier" ::: "memory");
    k5_la(fa[1], d.voffH, srdA, d.rot[1] * 4096); k5_la(fa[2], d.voffH, srdA, d.rot[2] * 4096);
    f32x4 acc[4][5] = {};
    const LAS char* pw = sm + w * 2048 + lane * 16; const LAS char* pwh; { unsigned a = sb + w * 2048 + lane * 16 + 65536; PIN(a); pwh = (const LAS char*)(size_t)a; }
    const LAS char* pwx = sm + L_W5X + w * 4096 + lane * 16;
    bf16x8 fl[5];
#define K5_STEP(J) do { \
      asm volatile("s_waitcnt vmcnt(%4)" : "+v"(fa[J][0]), "+v"(fa[J][1]), "+v"(fa[J][2]), "+v"(fa[J][3]) : "i"(J < 5 ? 8 : J == 5 ? 4 : 0) : "memory"); \
      _Pragma("unroll") for (int cc = 0; cc < 5; ++cc) { \
        _Pragma("unroll") for (int r = 0; r < 4; ++r) acc[r][cc] = MFMA16(J < 4 ? d.wr[(J < 4 ? J : 0) * 5 + cc] : fl[cc], fa[J][r], acc[r][cc]); \
        if (J >= 3 && J < 6) { const int pp = (J - 3) * 5 + cc; fl[cc] = lds_rd(pp == 14 ? pwx : w5_off(pp) < 65536 ? pw + w5_off(pp) : pwh + (w5_off(pp) - 65536)); } \
        SBAR(); } \
      if (J + 3 < 7) k5_la(fa[J + 3 < 7 ? J + 3 : 0], d.voffH, srdA, d.rot[J + 3 < 7 ? J + 3 : 0] * 4096); \
      SBAR(); } while (0)
    K5_STEP(0); K5_STEP(1); K5_STEP(2); K5_STEP(3); K5_STEP(4); K5_STEP(5); K5_STEP(6);
    STAMP(13);
    {
      const unsigned lo = q16 * 32 + (((g4 & 1) ^ ((q16 >> 2) & 1)) * 16);
      unsigned a0 = sb + w * 2048 + (g4 >> 1) * SLOT + lo, a1 = a0 + 65536, a2 = sb + L_OWN + w * 4096 + (g4 >> 1) * 2048 + lo;
      PIN(a0); PIN(a1); PIN(a2);
#pragma unroll
      for (int cc = 0; cc < 5; ++cc)
#pragma unroll
        for (int r = 0; r < 4; ++r) *(LAS f32x4*)((LAS char*)(size_t)((cc < 2 ? a0 : cc < 4 ? a1 : a2) ^ ((cc & 1) * 16)) + (cc & 1) * 2 * SLOT + r * 512) = acc[r][cc];
    }
    asm volatile("s_waitcnt lgkmcnt(0)\n\ts_barrier" ::: "memory");
    STAMP(14);
    const __amdgpu_buffer_rsrc_t srdOut = mk_srd(out, S * D * 2);
#pragma unroll
    for (int it = 0; it < 2; ++it) {
      if (it == 1 && lane >= 16) break;
      const int ch = it ? 8 + fch : fch, r = w * 8 + (lane & 7), sw = (((r >> 2) ^ (ch >> 1)) & 1) * 16;
      const LAS char* pr = sm + (it ? L_OWN + (fch & 1) * 2048 : fch * SLOT) + r * 32;
      f32x4 s0 = {}, s1 = {};
#pragma unroll
      for (int i = 0; i < 8; ++i) { s0 += *(const LAS f32x4*)(pr + i * (it ? 4096 : 2048) + sw); s1 += *(const LAS f32x4*)(pr + i * (it ? 4096 : 2048) + (sw ^ 16)); }
      const f32x4 g0 = gb[it][0], g1 = gb[it][1];
      u32x4 o;
      o[0] = pk2(bflo(x1[it][0]) + g0[0] * s0[0], bfhi(x1[it][0]) + g0[1] * s0[1]); o[1] = pk2(bflo(x1[it][1]) + g0[2] * s0[2], bfhi(x1[it][1]) + g0[3] * s0[3]);
      o[2] = pk2(bflo(x1[it][2]) + g1[0] * s1[0], bfhi(x1[it][2]) + g1[1] * s1[1]); o[3] = pk2(bflo(x1[it][3]) + g1[2] * s1[2], bfhi(x1[it][3]) + g1[3] * s1[3]);
      st16<0>(o, srdOut, (frow * D + nt * 80 + ch * 8) * 2);
    }
  }
  STAMP(15); STAMP_FLUSH(act4 ? 3 : 4, STAMP_WAVE);
}

extern "C" void kernel_launch(void* const* d_in, const int* in_sizes, int n_in, void* d_out, int out_size, void* d_ws, size_t ws_size, hipStream_t stream) {
  const u16* x = (const u16*)d_in[0]; const u16* kc = (const u16*)d_in[1]; const u16* vc = (const u16*)d_in[2];
  const u16* wq = (const u16*)d_in[3]; const u16* wo = (const u16*)d_in[4];
  const u16* wg = (const u16*)d_in[6]; const u16* wu = (const u16*)d_in[7]; const u16* wd = (const u16*)d_in[8];
  const u16* c = (const u16*)d_in[9]; const u16* wmod = (const u16*)d_in[10];
  char* ws = (char*)d_ws; u16* out = (u16*)d_out;
  constexpr int LDS2 = L2_SIDE + 32768;
  (void)hipFuncSetAttribute((const void*)k2_attention, hipFuncAttributeMaxDynamicSharedMemorySize, LDS2);
  (void)hipFuncSetAttribute((const void*)k3_tail, hipFuncAttributeMaxDynamicSharedMemorySize, LDS3);
  hipLaunchKernelGGL(k1_front, dim3(K1_LN + K1_MOD), dim3(256), 0, stream, x, c, wmod, ws);
  hipLaunchKernelGGL(k2_attention, dim3(256), dim3(512), LDS2, stream, wq, kc, vc, wo, wd, wg, wu, ws);
  hipLaunchKernelGGL(k3_tail, dim3(256), dim3(512), LDS3, stream, wg, wu, ws, out);
}
```

```cpp
#include <hip/hip_runtime.h>
#include <hip/hip_bf16.h>
#include <stdint.h>
#include <utility>

typedef __bf16   bf16x8 __attribute__((ext_vector_type(8)));
typedef __bf16   bf16x4 __attribute__((ext_vector_type(4)));
typedef __bf16   bf16x2 __attribute__((ext_vector_type(2)));
typedef short    s16x4  __attribute__((ext_vector_type(4)));
typedef float    f32x16 __attribute__((ext_vector_type(16)));
typedef float    f32x4  __attribute__((ext_vector_type(4)));
typedef float    f32x2  __attribute__((ext_vector_type(2)));
typedef unsigned u32x4  __attribute__((ext_vector_type(4)));
typedef unsigned u32x2  __attribute__((ext_vector_type(2)));
typedef unsigned short u16;
#define LAS __attribute__((address_space(3)))
#define DI __device__ __forceinline__
#define SBAR() __builtin_amdgcn_sched_barrier(0)
#define MFMA32(a, b, c) __builtin_amdgcn_mfma_f32_32x32x16_bf16((a), (b), (c), 0, 0, 0)
#define MFMA16(a, b, c) __builtin_amdgcn_mfma_f32_16x16x32_bf16((a), (b), (c), 0, 0, 0)
#define MFMA16_ACC(c, a, b) asm volatile("v_mfma_f32_16x16x32_bf16 %0, %1, %2, %0" : "+v"(c) : "v"(a), "v"(b))

#define WS_BASE 0
#define STAMP_DECL
#define STAMP(i)
#define STAMP_FLUSH(k, wv)

#define NT_SIDE 1
#define NT_K1 1
#define NT_KV 1
#define NT_G 0
#define WQ_MODE 0
constexpr int S = 2048, D = 640, NH = 32, DH = 128, SK = 2048, W = 512, F = 1728, DQ = NH * DH;
constexpr float EPS = 1e-5f;

constexpr size_t WS_MOD  = 0;
constexpr size_t WS_SC   = 28672;
constexpr size_t WS_LN   = 32768;
constexpr size_t WS_CSBS = 49152;
constexpr size_t WS_CNT  = 81920;
constexpr size_t WS_STAT = 196608;
constexpr size_t WS_MOD2 = 327680;
constexpr size_t WS_H    = 393216;
constexpr size_t WS_XT   = WS_H + (size_t)S * D * 2;
constexpr size_t WS_Y    = WS_XT + (size_t)S * D * 2;
constexpr size_t WS_O    = WS_Y + (size_t)S * D * 2;
constexpr size_t WS_ACT  = WS_O + (size_t)S * DQ * 2;
constexpr size_t WS_WOT  = WS_ACT + (size_t)S * F * 2;
constexpr size_t WS_WDT  = WS_WOT + (size_t)DQ * D * 2;
constexpr size_t WS_WGU  = WS_WDT + (size_t)F * D * 2;
constexpr size_t WS_PART = WS_WGU + (size_t)F * D * 4;
constexpr int CNT_P = 0, CNT_Y = 256, CNT_A = 512, CNT_C = 768;
static_assert(WS_CSBS + 27 * 1024 <= WS_CNT && WS_CNT + 896 * 128 <= WS_STAT && WS_STAT + (size_t)S * 64 <= WS_MOD2 && WS_MOD2 + 5 * 2560 * 4 <= WS_H, "ws map");

DI float bf2f(u16 v) { return __uint_as_float((unsigned)v << 16); }
DI float bflo(unsigned v) { return __uint_as_float(v << 16); }
DI float bfhi(unsigned v) { return __uint_as_float(v & 0xffff0000u); }
DI unsigned pk2(float lo, float hi) { f32x2 v = {lo, hi}; bf16x2 b = __builtin_convertvector(v, bf16x2); return __builtin_bit_cast(unsigned, b); }
DI float rbf(float x) { return bflo(pk2(x, 0.f)); }
DI unsigned h_off(int row, int cg) { return (((row >> 5) * 20 + (cg >> 2)) * 2 + ((row >> 4) & 1)) * 1024 + ((row & 15) + 16 * (cg & 3)) * 16; }
DI unsigned rfl(unsigned x) { return __builtin_amdgcn_readfirstlane(x); }
DI int swap23(int i) { return (i & ~12) | ((i & 4) << 1) | ((i & 8) >> 1); }

DI void dma16(unsigned m0v, unsigned voff, __amdgpu_buffer_rsrc_t srd, unsigned soff) {
  asm volatile("s_mov_b32 m0, %0\n\ts_nop 0\n\tbuffer_load_dwordx4 %1, %2, %3 offen lds" :: "s"(m0v), "v"(voff), "s"(srd), "s"(soff) : "memory");
}
template <int MO, int SO, bool NT = false> DI void dma16i(unsigned m0b, unsigned voff, __amdgpu_buffer_rsrc_t srd, unsigned sob) {
  unsigned t;
  if constexpr (NT) asm volatile("s_add_u32 m0, %1, %5\n\ts_add_u32 %0, %4, %6\n\tbuffer_load_dwordx4 %2, %3, %0 offen nt lds" : "=&s"(t) : "s"(m0b), "v"(voff), "s"(srd), "s"(sob), "i"(MO), "i"(SO) : "memory", "scc");
  else
  asm volatile("s_add_u32 m0, %1, %5\n\ts_add_u32 %0, %4, %6\n\tbuffer_load_dwordx4 %2, %3, %0 offen lds" : "=&s"(t) : "s"(m0b), "v"(voff), "s"(srd), "s"(sob), "i"(MO), "i"(SO) : "memory", "scc");
}
template <int OFF> DI void ld16(bf16x8& r, unsigned voff, __amdgpu_buffer_rsrc_t srd, unsigned soff) {
  asm volatile("buffer_load_dwordx4 %0, %1, %2, %3 offen offset:%4" : "=v"(r) : "v"(voff), "s"(srd), "s"(soff), "i"(OFF) : "memory");
}
template <int OFF> DI void ld16c(bf16x8& r, unsigned voff, __amdgpu_buffer_rsrc_t srd, unsigned soff) {
#define SC1S " sc1"
  asm volatile("buffer_load_dwordx4 %0, %1, %2, %3 offen offset:%4" SC1S : "=v"(r) : "v"(voff), "s"(srd), "s"(soff), "i"(OFF) : "memory");
}
template <int AUX> DI void st16(u32x4 v, __amdgpu_buffer_rsrc_t srd, unsigned voff) { __builtin_amdgcn_raw_buffer_store_b128(v, srd, voff, 0, AUX); }
DI unsigned umax4(const u32x4& v, unsigned m) { return max(max(max(m, v[0]), v[1]), max(v[2], v[3])); }
DI unsigned umax4(const bf16x8& v, unsigned m) { return umax4(__builtin_bit_cast(u32x4, v), m); }
DI bool pending(unsigned m) { return __builtin_expect(__any(m == 0xffffffffu), 0); }
DI u32x4 nopoison(u32x4 v) { return (u32x4){min(v[0], 0xfffffffeu), min(v[1], 0xfffffffeu), min(v[2], 0xfffffffeu), min(v[3], 0xfffffffeu)}; }
constexpr unsigned SPIN_CAP = 1u << 16;
DI unsigned umax16(const bf16x8& a, const bf16x8& b, const bf16x8& c, const bf16x8& d) {
  const u32x4 p = __builtin_bit_cast(u32x4, a), q = __builtin_bit_cast(u32x4, b), r = __builtin_bit_cast(u32x4, c), t = __builtin_bit_cast(u32x4, d);
  auto m3 = [](unsigned x, unsigned y, unsigned z) { return max(max(x, y), z); };
  unsigned m = m3(p[0], p[1], p[2]), n = m3(p[3], q[0], q[1]);
  m = m3(m, q[2], q[3]); n = m3(n, r[0], r[1]); m = m3(m, r[2], r[3]); n = m3(n, t[0], t[1]); m = m3(m, t[2], t[3]);
  return max(m, n);
}
template <int STEP> DI void reload4(unsigned mx, bf16x8& a, bf16x8& b, bf16x8& c, bf16x8& d, unsigned voff, __amdgpu_buffer_rsrc_t srd, unsigned soff) {
  if (pending(mx)) {
    unsigned spins = 0;
    do asm volatile("buffer_load_dwordx4 %0, %4, %5, %6 offen sc1\n\tbuffer_load_dwordx4 %1, %4, %5, %6 offen offset:%7 sc1\n\t"
                    "buffer_load_dwordx4 %2, %4, %5, %6 offen offset:%8 sc1\n\tbuffer_load_dwordx4 %3, %4, %5, %6 offen offset:%9 sc1\n\ts_waitcnt vmcnt(0)"
                    : "+v"(a), "+v"(b), "+v"(c), "+v"(d) : "v"(voff), "s"(srd), "s"(soff), "i"(STEP), "i"(2 * STEP), "i"(3 * STEP) : "memory");
    while (__any(umax16(a, b, c, d) == 0xffffffffu) && ++spins < SPIN_CAP);
  }
}
DI void first8(bf16x8 (&a)[4], bf16x8 (&b)[4], unsigned voff, __amdgpu_buffer_rsrc_t srd, unsigned soa, unsigned sob) {
  unsigned spins = 0;
  do asm volatile("buffer_load_dwordx4 %0, %8, %9, %10 offen sc1\n\tbuffer_load_dwordx4 %1, %8, %9, %10 offen offset:1024 sc1\n\t"
                  "buffer_load_dwordx4 %2, %8, %9, %10 offen offset:2048 sc1\n\tbuffer_load_dwordx4 %3, %8, %9, %10 offen offset:3072 sc1\n\t"
                  "buffer_load_dwordx4 %4, %8, %9, %11 offen sc1\n\tbuffer_load_dwordx4 %5, %8, %9, %11 offen offset:1024 sc1\n\t"
                  "buffer_load_dwordx4 %6, %8, %9, %11 offen offset:2048 sc1\n\tbuffer_load_dwordx4 %7, %8, %9, %11 offen offset:3072 sc1\n\ts_waitcnt vmcnt(4)"
                  : "=&v"(a[0]), "=&v"(a[1]), "=&v"(a[2]), "=&v"(a[3]), "=&v"(b[0]), "=&v"(b[1]), "=&v"(b[2]), "=&v"(b[3]) : "v"(voff), "s"(srd), "s"(soa), "s"(sob) : "memory");
  while (__any(umax16(a[0], a[1], a[2], a[3]) == 0xffffffffu) && ++spins < SPIN_CAP);
}
template <int N> DI void wait_bar4(bf16x8& a, bf16x8& b, bf16x8& c, bf16x8& d) {
  asm volatile("s_waitcnt vmcnt(%4) lgkmcnt(0)\n\ts_barrier" : "+v"(a), "+v"(b), "+v"(c), "+v"(d) : "i"(N) : "memory");
}
template <int N> DI void wait_bar() { asm volatile("s_waitcnt vmcnt(%0) lgkmcnt(0)\n\ts_barrier" :: "i"(N) : "memory"); }
DI bf16x8 lds_rd(const LAS char* p) { return *(const LAS bf16x8*)p; }
DI bf16x8 lds_tr(const LAS char* p0, const LAS char* p1) {
  s16x4 lo = __builtin_amdgcn_ds_read_tr16_b64_v4i16((LAS s16x4*)p0);
  s16x4 hi = __builtin_amdgcn_ds_read_tr16_b64_v4i16((LAS s16x4*)p1);
  return __builtin_bit_cast(bf16x8, __builtin_shufflevector(lo, hi, 0, 1, 2, 3, 4, 5, 6, 7));
}
DI bf16x8 pack8(const f32x16& x, int s, float m) {
  u32x4 w = {pk2(x[8 * s] * m, x[8 * s + 1] * m), pk2(x[8 * s + 2] * m, x[8 * s + 3] * m), pk2(x[8 * s + 4] * m, x[8 * s + 5] * m), pk2(x[8 * s + 6] * m, x[8 * s + 7] * m)};
  return __builtin_bit_cast(bf16x8, w);
}
DI __amdgpu_buffer_rsrc_t mk_srd(const void* p, unsigned bytes) { return __builtin_amdgcn_make_buffer_rsrc((void*)p, (short)0, (int)bytes, 0x00020000); }

constexpr int K1_MOD = 104, K1_LN = 128;
__global__ __launch_bounds__(256) void k1_front(const u16* __restrict__ x, const u16* __restrict__ c, const u16* __restrict__ wmod, const u16* __restrict__ wq, char* __restrict__ ws) {
  __shared__ float red[4][64];
  const int tid = threadIdx.x, bid = blockIdx.x, lane = tid & 63, wv = tid >> 6;
  STAMP_DECL; STAMP(0);
#define K1_WROWS 12
  auto pull_wq = [&]() {
    const unsigned vo = ((bid & 7) >> 1) * 2048 + (tid & 31) * 64; const __amdgpu_buffer_rsrc_t sq = mk_srd(wq, D * DQ * 2);
    if (WQ_MODE == 2) return 0u;
    if (WQ_MODE == 1) { const unsigned r = (bid >> 3) * 24 + (tid >> 5);
      return __builtin_amdgcn_raw_buffer_load_b32(sq, r * 8192 + vo, 0, 2) ^ __builtin_amdgcn_raw_buffer_load_b32(sq, (r + 8) * 8192 + vo, 0, 2) ^ __builtin_amdgcn_raw_buffer_load_b32(sq, (r + 16) * 8192 + vo, 0, 2); }
    const unsigned r = ((bid >> 3) * 2 + (bid & 1)) * 12 + (tid >> 5);
    return __builtin_amdgcn_raw_buffer_load_b32(sq, r * 8192 + vo, 0, 0) ^ __builtin_amdgcn_raw_buffer_load_b32(sq, tid < 128 ? (r + 8) * 8192 + vo : 0x7fffffffu, 0, 0);
  };
  if (bid < K1_MOD) {
    if (bid >= 100) { asm volatile("" :: "v"(pull_wq())); return; }
    const int sl = bid / 5, kh = bid - sl * 5, ch = tid & 7, rg = tid >> 3;
    u32x4 wv4[4]; u16 cv[4];
#pragma unroll
    for (int i = 0; i < 4; ++i) { const u32x4* q = (const u32x4*)(wmod + (size_t)(kh * 128 + rg + 32 * i) * (6 * D) + sl * 64 + ch * 8); wv4[i] = NT_K1 ? __builtin_nontemporal_load(q) : *q; }
#pragma unroll
    for (int i = 0; i < 4; ++i) cv[i] = c[kh * 128 + rg + 32 * i];
    SBAR(); const unsigned pq = pull_wq(); SBAR();
    float acc[8] = {0, 0, 0, 0, 0, 0, 0, 0};
#pragma unroll
    for (int i = 0; i < 4; ++i) {
      const float a = bf2f(cv[i]), sv = rbf(a * __builtin_amdgcn_rcpf(1.f + __expf(-a)));
      if (sl == 0 && ch == 0) ((float*)(ws + WS_SC))[kh * 128 + rg + 32 * i] = sv;
#pragma unroll
      for (int e = 0; e < 4; ++e) { acc[2 * e] += sv * bflo(wv4[i][e]); acc[2 * e + 1] += sv * bfhi(wv4[i][e]); }
    }
#pragma unroll
    for (int e = 0; e < 8; ++e) { acc[e] += __shfl_xor(acc[e], 8); acc[e] += __shfl_xor(acc[e], 16); acc[e] += __shfl_xor(acc[e], 32); }
    if (lane < 8) { *(f32x4*)&red[wv][ch * 8] = (f32x4){acc[0], acc[1], acc[2], acc[3]}; *(f32x4*)&red[wv][ch * 8 + 4] = (f32x4){acc[4], acc[5], acc[6], acc[7]}; }
    __syncthreads();
    if (tid < 64) ((float*)(ws + WS_MOD))[kh * 2 * D + sl * 64 + tid] = (red[0][tid] + red[1][tid]) + (red[2][tid] + red[3][tid]);
    asm volatile("" :: "v"(pq));
    STAMP(2); STAMP_FLUSH(1, 0);
    return;
  }
  if (tid < 7) *(unsigned*)(ws + WS_CNT + ((bid - K1_MOD) * 7 + tid) * 128) = 0u;
  const int row = (bid - K1_MOD) * 16 + wv * 4 + (lane >> 4), cg = lane & 15;
  u32x4 xv[5];
#pragma unroll
  for (int i = 0; i < 5; ++i) { const u32x4* q = (const u32x4*)(x + (size_t)row * D + (cg + 16 * i) * 8); xv[i] = NT_K1 ? __builtin_nontemporal_load(q) : *q; }
  SBAR(); const unsigned pq = pull_wq();
  u32x4 pf = {0, 0, 0, 0};
  {
    size_t pc; int d0, d1;
    asm volatile("s_getpc_b64 %0\nadaln_pc_here:\n\ts_mov_b32 %1, adaln_code_begin-adaln_pc_here\n\ts_mov_b32 %2, adaln_code_end-adaln_pc_here" : "=s"(pc), "=s"(d0), "=s"(d1));
    const __amdgpu_buffer_rsrc_t srdC = mk_srd((const void*)((pc + d0) & ~(size_t)15), (d1 - d0) & ~15);
    pf = __builtin_amdgcn_raw_buffer_load_b128(srdC, ((bid - K1_MOD) >> 3) * 8192 + tid * 32, 0, 0);
  }
#pragma unroll
  for (int i = 0; i < 5; ++i) *(u32x4*)(ws + WS_H + (size_t)(bid - K1_MOD) * 20480 + i * 4096 + tid * 16) = (u32x4){~0u, ~0u, ~0u, ~0u};
#pragma unroll
  for (int i = 0; i < 5; ++i) *(u32x4*)(ws + WS_XT + h_off(row, cg + 16 * i)) = xv[i];
  float s = 0.f;
#pragma unroll
  for (int i = 0; i < 5; ++i)
#pragma unroll
    for (int e = 0; e < 4; ++e) s += bflo(xv[i][e]) + bfhi(xv[i][e]);
  s += __shfl_xor(s, 1); s += __shfl_xor(s, 2); s += __shfl_xor(s, 4); s += __shfl_xor(s, 8);
  const float m = s * (1.f / D);
  float q = 0.f;
#pragma unroll
  for (int i = 0; i < 5; ++i)
#pragma unroll
    for (int e = 0; e < 4; ++e) { const float a = bflo(xv[i][e]) - m, b = bfhi(xv[i][e]) - m; q += a * a + b * b; }
  q += __shfl_xor(q, 1); q += __shfl_xor(q, 2); q += __shfl_xor(q, 4); q += __shfl_xor(q, 8);
  if (cg == 0) *(f32x2*)(ws + WS_LN + row * 8) = (f32x2){m, rsqrtf(q * (1.f / D) + EPS)};
  asm volatile("" :: "v"(pf), "v"(pq));
  STAMP(1); STAMP_FLUSH(1, 0);
}

constexpr int SLOT = 16384, NSLOT = 8;
constexpr float QSCALE = 0.08838834764831845f * 1.4426950408889634f;
constexpr float RTHR = 64.f;

#define PIN(x) asm volatile("" : "+v"(x))
#define EX2(x) __builtin_amdgcn_exp2f(x)
template <int N> DI void wait_bar4v(bf16x8& a, bf16x8& b, bf16x8& c, bf16x8& d) {
  asm volatile("s_waitcnt vmcnt(%4)\n\ts_barrier" : "+v"(a), "+v"(b), "+v"(c), "+v"(d) : "i"(N) : "memory");
}
template <int N> DI void wait_barv() { asm volatile("s_waitcnt vmcnt(%0)\n\ts_barrier" :: "i"(N) : "memory"); }
DI const LAS char* slot_ptr(unsigned lanebase, int tile) { unsigned a = lanebase + (tile & (NSLOT - 1)) * SLOT; PIN(a); return (const LAS char*)(size_t)a; }
DI float sum1632(float v) {
  { auto r = __builtin_amdgcn_permlane16_swap(__float_as_uint(v), __float_as_uint(v), false, false); v = __uint_as_float(r[0]) + __uint_as_float(r[1]); }
  { auto r = __builtin_amdgcn_permlane32_swap(__float_as_uint(v), __float_as_uint(v), false, false); v = __uint_as_float(r[0]) + __uint_as_float(r[1]); }
  return v;
}
DI float xor8(float v) { return __int_as_float(__builtin_amdgcn_update_dpp(0, __float_as_int(v), 0x128, 0xf, 0xf, true)); }
DI float sum816(float v) { return sum1632(v + xor8(v)); }
DI float max1632(float v) {
  { auto r = __builtin_amdgcn_permlane16_swap(__float_as_uint(v), __float_as_uint(v), false, false); v = __builtin_fmaxf(__uint_as_float(r[0]), __uint_as_float(r[1])); }
  { auto r = __builtin_amdgcn_permlane32_swap(__float_as_uint(v), __float_as_uint(v), false, false); v = __builtin_fmaxf(__uint_as_float(r[0]), __uint_as_float(r[1])); }
  return v;
}
DI bf16x8 pack44(const f32x4& a, const f32x4& b, float m) { return __builtin_bit_cast(bf16x8, (u32x4){pk2(a[0] * m, a[1] * m), pk2(a[2] * m, a[3] * m), pk2(b[0] * m, b[1] * m), pk2(b[2] * m, b[3] * m)}); }

constexpr int S2 = 16416, NS2 = 7, L2_SIDE = NS2 * S2;
struct K2C {
  __amdgpu_buffer_rsrc_t srdW, srdK, srdV, srdH;
  unsigned voffW, voffK, voffV, voffH, m0T, m0K, sofW, sofKV, sofH, aT, aK;
  const LAS char *pT, *pK;
  bf16x8 f[3][4];
  bf16x8 qf[4][2];
  f32x4 o[8][2], sA[2][2], sB[2][2], negm[2];
  u32x4 pA[2], pB[2];
  float l[2], fp[2], aone; int pend, mx[2]; unsigned chk;
  __amdgpu_buffer_rsrc_t srdSs, srdSd;
  __amdgpu_buffer_rsrc_t srdGs, srdGd; unsigned voffG, sofGs, voffGd; const LAS char* pG;
  __amdgpu_buffer_rsrc_t srdT; unsigned sofT2, m0X;
  __amdgpu_buffer_rsrc_t srdM, srdC; unsigned voffM, voffC, voffMs; bf16x8 mw[2]; f32x2 msc; float mv[8]; LAS f32x2* pM; const LAS float* pMr;
  unsigned voffS, sofSs, sofSd[4], m0S; const LAS char* pS; bf16x8 sf[2];
};
DI const LAS char* ring_ptr(unsigned lanebase, int tile) { unsigned a = lanebase + (tile % NS2) * S2; PIN(a); return (const LAS char*)(size_t)a; }
template <int I, int HALF> DI void k2_dma1(const K2C& c) {
  constexpr int o = (I % NS2) * S2 + HALF * 1024;
  if constexpr (I < 10) dma16i<o, I * 64 * 8192 + HALF * 128, WQ_MODE == 2>(c.m0T, c.voffW, c.srdW, c.sofW);
  else if constexpr (((I - 10) & 1) == 0) dma16i<o, ((I - 10) >> 1) * 16384 + HALF * 128, NT_KV>(c.m0K, c.voffK, c.srdK, c.sofKV);
  else dma16i<o, ((I - 10) >> 1) * 16384 + HALF * 128, NT_KV>(c.m0T, c.voffV, c.srdV, c.sofKV);
}
template <int I> DI void k2_dma(const K2C& c) { k2_dma1<I, 0>(c); k2_dma1<I, 1>(c); }
template <int I, int S_> DI void k2_ldh1(const K2C& c, bf16x8& r) { ld16c<S_ * 1024>(r, c.voffH, c.srdH, c.sofH + I * 4096); }
template <int H> DI void rdT(bf16x8& f, const LAS char* b, int ks, int dt) {
  const s16x4 v = __builtin_amdgcn_ds_read_tr16_b64_v4i16((LAS s16x4*)(b + ks * 8208 + (dt >> 1) * 512 + (dt & 1) * 8 + H * 256));
  u32x4 t = H ? __builtin_bit_cast(u32x4, f) : u32x4{}; const u32x2 x = __builtin_bit_cast(u32x2, v);
  if constexpr (H == 0) t = __builtin_shufflevector(x, x, 0, 1, -1, -1); else { t[2] = x[0]; t[3] = x[1]; }
  f = __builtin_bit_cast(bf16x8, t);
}
struct Quad { int kind, j, i; };
constexpr Quad quad_of(int Q) {
  if (Q < 40) return {0, Q >> 2, Q & 3};
  if (Q < 42) return {1, 0, Q - 40};
  const int T = (Q - 42) >> 3, r = (Q - 42) & 7;
  if (T < 7) return (r >> 1) == 0 ? Quad{1, 2 * T + 1, r & 1} : (r >> 1) == 1 ? Quad{2, 2 * T, r & 1} : (r >> 1) == 2 ? Quad{1, 2 * T + 2, r & 1} : Quad{2, 2 * T + 1, r & 1};
  return r < 2 ? Quad{1, 15, r} : r < 4 ? Quad{2, 14, r - 2} : r < 6 ? Quad{2, 15, r - 4} : Quad{3, 0, 0};
}
template <int Q, int M> DI void frag_ld(K2C& c) {
  constexpr Quad q = quad_of(Q); constexpr int E = M >> 1;
  if constexpr (M == 0 && q.i == 0) {
    if constexpr (q.kind == 0) c.pT = ring_ptr(c.aT, q.j);
    if constexpr (q.kind == 1 && (q.j & 1) == 0) c.pK = ring_ptr(c.aK, 10 + q.j);
    if constexpr (q.kind == 2 && (q.j & 1) == 0) c.pT = ring_ptr(c.aT, 11 + q.j);
  }
  if constexpr (q.kind == 0) rdT<(M & 1)>(c.f[Q % 3][E], c.pT, E & 1, 2 * q.i + (E >> 1));
  if constexpr (q.kind == 1 && (M & 1) == 0) c.f[Q % 3][E] = lds_rd(c.pK + (q.j & 1) * 8192 + q.i * 256 + E * 512);
  if constexpr (q.kind == 2) rdT<(M & 1)>(c.f[Q % 3][E], c.pT, q.j & 1, 4 * q.i + E);
}
template <int Q, int... Ms> DI void frag_ld_all(K2C& c, std::integer_sequence<int, Ms...>) { (frag_ld<Q, Ms>(c), ...); }
DI int imx3(int a, float b, float c) { return max(max(a, __float_as_int(b)), __float_as_int(c)); }
template <int U, int PART> DI void sm_max(K2C& c, f32x4 (&s)[2][2]) {
  if constexpr (PART == 0) c.mx[U] = max(imx3(__float_as_int(s[0][U][0]), s[0][U][1], s[0][U][2]), __float_as_int(s[0][U][3]));
  else c.mx[U] = imx3(imx3(c.mx[U], s[1][U][0], s[1][U][1]), s[1][U][2], s[1][U][3]);
  PIN(c.mx[U]);
}
template <int E> DI void sm_exp(f32x4 (&s)[2][2]) { float x = s[E >> 3][(E >> 2) & 1][E & 3]; asm volatile("v_exp_f32 %0, %0" : "+v"(x)); s[E >> 3][(E >> 2) & 1][E & 3] = x; }
template <int E> DI void sm_add(K2C& c, f32x4 (&s)[2][2]) { asm volatile("v_add_f32 %0, %0, %1" : "+v"(c.l[(E >> 2) & 1]) : "v"(s[E >> 3][(E >> 2) & 1][E & 3])); }
template <int V> DI void sm_first(K2C& c, f32x4 (&s)[2][2]) {
  if constexpr (V == 1 || V == 2) sm_max<1, V - 1>(c, s);
  if constexpr (V == 3) {
    float t0, t1;
#define RMOVE(MX, F, L, S0, S1, NG) \
                 "v_mov_b32 %[t0], %[" MX "]\n\tv_mov_b32 %[t1], %[" MX "]\n\ts_nop 1\n\tv_permlane16_swap_b32 %[t0], %[t1]\n\ts_nop 1\n\tv_max_i32 %[t0], %[t0], %[t1]\n\t" \
                 "v_mov_b32 %[t1], %[t0]\n\ts_nop 1\n\tv_permlane32_swap_b32 %[t0], %[t1]\n\ts_nop 1\n\tv_max3_i32 %[t0], %[t0], %[t1], 0\n\t" \
                 "v_exp_f32 %[" F "], -%[t0]\n\tv_sub_f32 %[t1], 0, %[t0]\n\ts_nop 1\n\tv_mul_f32 %[" L "], %[" L "], %[" F "]\n\t" \
                 "v_mfma_f32_16x16x4_f32 %[" S0 "], %[one], %[t1], %[" S0 "]\n\tv_mfma_f32_16x16x4_f32 %[" S1 "], %[one], %[t1], %[" S1 "]\n\tv_mfma_f32_16x16x4_f32 %[" NG "], %[one], %[t1], %[" NG "]\n\t"
    asm volatile("v_max_i32 %[t0], %[m0], %[m1]\n\ts_mov_b32 %[pend], 0\n\tv_cmp_lt_i32 vcc, %[thr], %[t0]\n\ts_cbranch_vccz 1f\n\t"
                 RMOVE("m0", "f0", "l0", "s00", "s10", "n0") RMOVE("m1", "f1", "l1", "s01", "s11", "n1")
                 "s_mov_b32 %[pend], 1\n\ts_nop 15\n1:"
                 : [s00] "+v"(s[0][0]), [s01] "+v"(s[0][1]), [s10] "+v"(s[1][0]), [s11] "+v"(s[1][1]), [n0] "+v"(c.negm[0]), [n1] "+v"(c.negm[1]),
                   [l0] "+v"(c.l[0]), [l1] "+v"(c.l[1]), [f0] "+v"(c.fp[0]), [f1] "+v"(c.fp[1]), [pend] "=&s"(c.pend), [t0] "=&v"(t0), [t1] "=&v"(t1)
                 : [m0] "v"(c.mx[0]), [m1] "v"(c.mx[1]), [thr] "s"(__float_as_int(RTHR)), [one] "v"(c.aone) : "vcc", "memory");
#undef RMOVE
  }
  if constexpr (V >= 6) sm_add<V - 6>(c, s);
  if constexpr (V >= 4) sm_exp<V - 4>(s);
}
template <int V> DI void sm_second(K2C& c, f32x4 (&s)[2][2], u32x4 (&p)[2], f32x4 (&n)[2][2], bool next) {
  if constexpr (V < 6) sm_add<10 + V>(c, s);
  if constexpr (V < 4) sm_exp<12 + V>(s);
  if constexpr (V >= 4 && V < 12) { constexpr int e = V - 4, kt = e >> 2, u = (e >> 1) & 1; unsigned r;
    asm volatile("v_cvt_pk_bf16_f32 %0, %1, %2" : "=v"(r) : "v"(s[kt][u][2 * (e & 1)]), "v"(s[kt][u][2 * (e & 1) + 1])); p[u][2 * kt + (e & 1)] = r; }
  if constexpr (V >= 14) { if (next) sm_max<0, V - 14>(c, n); }
}
DI void apply_pend(K2C& c) {
  asm volatile("" ::: "memory");
  if (__builtin_expect(c.pend != 0, 0)) {
    asm volatile("s_nop 7" : "+v"(c.o[6][0]), "+v"(c.o[6][1]), "+v"(c.o[7][0]), "+v"(c.o[7][1]), "+v"(c.o[5][0]), "+v"(c.o[5][1]), "+v"(c.o[4][0]), "+v"(c.o[4][1]));
#pragma unroll
    for (int dt = 0; dt < 8; ++dt) { c.o[dt][0] *= c.fp[0]; c.o[dt][1] *= c.fp[1]; }
  }
}
enum { OP_NONE, OP_H, OP_D, OP_SL, OP_SS, OP_GL, OP_GS, OP_T, OP_ML, OP_MS };
struct MemOp { int kind, a, b; };
constexpr int PER(int t, int x) { return (42 + 8 * t) * 8 + x; }
constexpr MemOp mem_op(int z) {
  if (z < 320) {
    const int I = z >> 5, X = z & 31;
    if (I == 0) { if ((X & 3) == 1) return {OP_H, 2 + (X >> 4), (X >> 2) & 3}; if (X == 27 || X == 31) return {OP_D, 3, X == 31}; return {OP_NONE, 0, 0}; }
    if (X < 16 && (X & 3) == 1) { if (I < 7) return {OP_H, I + 3, X >> 2}; if (I == 7) return {OP_D, 10 + (X >> 3), (X >> 2) & 1}; if (I == 8 && X < 8) return {OP_D, 13, X >> 2}; }
    if (X == 19 || X == 27) return {OP_D, I < 7 ? I + 3 : I == 7 ? 12 : I + 6, X == 27};
    return {OP_NONE, 0, 0};
  }
  if (z < PER(0, 0)) return {OP_NONE, 0, 0};
  const int T = (z - PER(0, 0)) >> 6, X = (z - PER(0, 0)) & 63;
  if (X == 9 || X == 12) { if (T == 0) return {OP_D, 16, X == 12}; if (T <= 5) return {OP_D, 15 + 2 * T, X == 12}; }
  if ((X == 13 || X == 16) && T >= 1 && T <= 4) return {OP_D, 16 + 2 * T, X == 16};
  if (z == PER(0, 41) || z == PER(1, 10) || z == PER(1, 44) || z == PER(2, 10)) return {OP_SL, z == PER(0, 41) ? 0 : z == PER(1, 10) ? 1 : z == PER(1, 44) ? 2 : 3, 0};
  if (T == 3 && (X == 49 || X == 51 || X == 57 || X == 59)) return {OP_SS, X == 49 ? 0 : X == 51 ? 1 : X == 57 ? 2 : 3, 0};
  if ((T == 4 || T == 5) && (X == 10 || X == 41)) return {OP_GL, 2 * (T - 4) + (X == 41), 0};
  if (T == 7 && (X == 17 || X == 19 || X == 25 || X == 27)) return {OP_GS, X == 17 ? 0 : X == 19 ? 1 : X == 25 ? 2 : 3, 0};
  if (z == PER(2, 41) || z == PER(2, 44) || z == PER(3, 10)) return {OP_ML, z == PER(2, 41) ? 0 : z == PER(2, 44) ? 1 : 2, 0};
  if (z == PER(6, 16)) return {OP_MS, 0, 0};
  if (T == 6 && X >= 41 && X <= 45) return {OP_T, X - 41, 0};
  return {OP_NONE, 0, 0};
}
constexpr int younger(int z, int kind, int a) { int n = 0; for (int y = z - 1; y >= 0; --y) { const MemOp o = mem_op(y); if (o.kind == kind && o.a == a) return n; n += o.kind != OP_NONE; } return n; }
constexpr int imin(int a, int b) { return a < b ? a : b; }
constexpr int bq_wait(int i) { return imin(younger(32 * i + 16, OP_H, i + 1), younger(32 * i + 16, OP_D, i + 1)); }
constexpr int ba_wait(int t) { return imin(younger(PER(t, 0), OP_D, 11 + 2 * t), t < 7 ? younger(PER(t, 0), OP_D, 12 + 2 * t) : 63); }
static_assert(bq_wait(0) == 4 && bq_wait(3) == 10 && bq_wait(9) == 8 && ba_wait(0) == 6, "schedule");
template <int P> DI void side_ld(const K2C& c) { dma16i<P * 1024, P * 8 * D * 2, NT_SIDE>(c.m0S, c.voffS, c.srdSs, c.sofSs); }
template <int P> DI void side_rd(K2C& c) { const LAS char* p = c.pS + (P >> 1) * 512 + (P & 1) * 32; c.sf[P & 1] = lds_tr(p, p + 256); }
template <int P> DI void side_st(const K2C& c) { st16<16>(__builtin_bit_cast(u32x4, c.sf[P & 1]), c.srdSd, c.voffH + c.sofSd[P]); }
template <int P> DI void side2_ld(const K2C& c) { dma16i<P * 1024, P * 8 * F * 2, NT_G>(c.m0S, c.voffG, c.srdGs, c.sofGs); }
template <int P> DI void side2_rd(K2C& c) { const LAS char* p = c.pG + (P >> 1) * 512 + (P & 1) * 8; c.sf[P & 1] = lds_tr(p, p + 256); }
template <int P> DI void side2_st(const K2C& c) { st16<16>(__builtin_bit_cast(u32x4, c.sf[P & 1]), c.srdGd, c.voffGd + (P >> 1) * 8192 + (P & 1) * 2048); }
template <int Q, int M> DI void k2_mem(K2C& c, bf16x8 (&hf)[10][4]) {
  constexpr int z = Q * 8 + M; constexpr MemOp o = mem_op(z);
  if constexpr (o.kind == OP_H) k2_ldh1<o.a, o.b>(c, hf[o.a][o.b]);
  if constexpr (o.kind == OP_D) k2_dma1<o.a, o.b>(c);
  if constexpr (o.kind == OP_SL) side_ld<o.a>(c);
  if constexpr (o.kind == OP_SS) side_st<o.a>(c);
  if constexpr (o.kind == OP_GL) side2_ld<o.a>(c);
  if constexpr (o.kind == OP_GS) side2_st<o.a>(c);
  if constexpr (o.kind == OP_T) dma16i<0, o.a * 1024>(c.m0X, c.voffH, c.srdT, c.sofT2);
  if constexpr (z == PER(3, 42)) { asm volatile("s_waitcnt vmcnt(%0)" :: "i"(younger(z + 1, OP_SL, 3)) : "memory"); side_rd<0>(c); }
  if constexpr (z == PER(3, 43)) side_rd<1>(c);
  if constexpr (z == PER(3, 49)) side_rd<2>(c);
  if constexpr (z == PER(3, 51)) side_rd<3>(c);
  if constexpr (z == PER(7, 10)) { asm volatile("s_waitcnt vmcnt(%0)" :: "i"(younger(z + 1, OP_GL, 3)) : "memory"); side2_rd<0>(c); }
  if constexpr (z == PER(7, 11)) side2_rd<1>(c);
  if constexpr (z == PER(7, 17)) side2_rd<2>(c);
  if constexpr (z == PER(7, 19)) side2_rd<3>(c);
  if constexpr (o.kind == OP_ML) {
    if constexpr (o.a == 0) asm volatile("buffer_load_dwordx2 %0, %1, %2, 0 offen" : "=v"(c.msc) : "v"(c.voffC), "s"(c.srdC) : "memory");
    else if constexpr (NT_SIDE) asm volatile("buffer_load_dwordx4 %0, %1, %2, %3 offen nt" : "=v"(c.mw[o.a - 1]) : "v"(c.voffM), "s"(c.srdM), "s"((o.a - 1) * 6 * D * 2) : "memory");
    else ld16<0>(c.mw[o.a - 1], c.voffM, c.srdM, (o.a - 1) * 6 * D * 2);
  }
  if constexpr (z == PER(5, 6)) asm volatile("s_waitcnt vmcnt(%3)" : "+v"(c.msc), "+v"(c.mw[0]), "+v"(c.mw[1]) : "i"(younger(z + 1, OP_ML, 2)) : "memory");
  if constexpr (z >= PER(5, 6) && z < PER(5, 14)) {
    constexpr int e = z - PER(5, 6); const unsigned a = __builtin_bit_cast(u32x4, c.mw[0])[e >> 1], b = __builtin_bit_cast(u32x4, c.mw[1])[e >> 1];
    c.mv[e] = (e & 1) ? c.msc[0] * bfhi(a) + c.msc[1] * bfhi(b) : c.msc[0] * bflo(a) + c.msc[1] * bflo(b); PIN(c.mv[e]);
  }
  if constexpr (z >= PER(5, 38) && z < PER(5, 42)) { constexpr int e = z - PER(5, 38);
    auto r = __builtin_amdgcn_permlane32_swap(__float_as_uint(c.mv[e]), __float_as_uint(c.mv[4 + e]), false, false); c.mv[e] = __uint_as_float(r[0]) + __uint_as_float(r[1]); PIN(c.mv[e]); }
  if constexpr (z >= PER(5, 42) && z < PER(5, 44)) { constexpr int e = z - PER(5, 42);
    auto r = __builtin_amdgcn_permlane16_swap(__float_as_uint(c.mv[e]), __float_as_uint(c.mv[2 + e]), false, false); c.mv[e] = __uint_as_float(r[0]) + __uint_as_float(r[1]); PIN(c.mv[e]); }
  if constexpr (z == PER(5, 45)) *c.pM = (f32x2){c.mv[0], c.mv[1]};
  if constexpr (z == PER(6, 6)) { c.mv[0] = c.pMr[0]; c.mv[1] = c.pMr[512]; }
  if constexpr (o.kind == OP_MS) __builtin_amdgcn_raw_buffer_store_b32(__float_as_uint(sum816(c.mv[0] + c.mv[1])), c.srdC, c.voffMs, 0, 0);
}
template <int SI, int M> DI void q_pack(K2C& c) {
  constexpr int t = M >> 2, u = (M >> 1) & 1, e = M & 1; unsigned r;
  asm volatile("v_cvt_pk_bf16_f32 %0, %1, %2" : "=v"(r) : "v"(c.o[2 * SI + t][u][2 * e]), "v"(c.o[2 * SI + t][u][2 * e + 1]));
  u32x4 v = __builtin_bit_cast(u32x4, c.qf[SI][u]); v[2 * t + e] = r; c.qf[SI][u] = __builtin_bit_cast(bf16x8, v);
}
template <int... Ms> DI void q_pack_last(K2C& c, std::integer_sequence<int, Ms...>) { (q_pack<3, Ms>(c), ...); }
template <int Q, int M> DI void k2_slot(K2C& c, bf16x8 (&hf)[10][4]) {
  constexpr Quad q = quad_of(Q);
  bf16x8 (&f)[4] = c.f[Q % 3];
  if constexpr (q.kind == 0) { constexpr int dt = 2 * q.i + (M >> 2), u = (M >> 1) & 1, ks = M & 1; MFMA16_ACC(c.o[dt][u], f[2 * (M >> 2) + ks], hf[q.j][2 * ks + u]); }
  if constexpr (q.kind == 1) { constexpr int u = M >> 2, e = M & 3; f32x4& s = (q.j & 1) ? c.sB[q.i][u] : c.sA[q.i][u];
    if constexpr (e == 0) asm volatile("v_mfma_f32_16x16x32_bf16 %0, %1, %2, %3" : "=&v"(s) : "v"(f[0]), "v"(c.qf[0][u]), "v"(c.negm[u])); else MFMA16_ACC(s, f[e], c.qf[e][u]); }
  if constexpr (q.kind == 2) { constexpr int e = M >> 1, u = M & 1;
    if constexpr (q.j == 0) asm volatile("v_mfma_f32_16x16x32_bf16 %0, %1, %2, 0" : "=&v"(c.o[4 * q.i + e][u]) : "v"(f[e]), "v"(c.pA[u]));
    else MFMA16_ACC(c.o[4 * q.i + e][u], f[e], (q.j & 1) ? c.pB[u] : c.pA[u]); }
  SBAR();
  frag_ld<Q + 2, M>(c);
  k2_mem<Q, M>(c, hf);
  if constexpr (q.kind == 0 && q.i == 2 && q.j < 9) {
    const u32x4 t = __builtin_bit_cast(u32x4, hf[q.j + 1][M >> 1]);
    if constexpr (M == 0) asm volatile("v_max_u32 %0, %1, %2" : "=v"(c.chk) : "v"(t[0]), "v"(t[1]));
    else asm volatile("v_max3_u32 %0, %0, %1, %2" : "+v"(c.chk) : "v"(t[2 * (M & 1)]), "v"(t[2 * (M & 1) + 1]));
  }
  if constexpr (Q >= 37 && Q < 40) q_pack<Q - 37, M>(c);
  if constexpr (Q >= 42) {
    if constexpr (q.kind == 1) { if constexpr (q.j & 1) sm_second<8 * q.i + M>(c, c.sA, c.pA, c.sB, true); else sm_second<8 * q.i + M>(c, c.sB, c.pB, c.sA, true); }
    if constexpr (q.kind == 2 && q.j < 15) { if constexpr (q.j & 1) sm_first<8 * q.i + M>(c, c.sA); else sm_first<8 * q.i + M>(c, c.sB); }
  }
  SBAR();
}
template <int... Vs> DI void sm_second_all(K2C& c, std::integer_sequence<int, Vs...>) { (sm_second<Vs>(c, c.sB, c.pB, c.sA, false), ...); }
#define PR1 3
#define PR2 6
#define PR3 7
template <int Q> DI void k2_quad(K2C& c, bf16x8 (&hf)[10][4]) {
  constexpr Quad q = quad_of(Q);
  if constexpr (q.kind == 0 && q.i == 2) {
    if constexpr (q.j < 9) wait_bar4v<bq_wait(q.j)>(hf[q.j + 1][0], hf[q.j + 1][1], hf[q.j + 1][2], hf[q.j + 1][3]);
    else wait_barv<bq_wait(9)>();
  }
  if constexpr (q.kind == 0 && q.i == 3 && q.j < 9) {
    asm volatile("" ::: "memory");
    reload4<1024>(c.chk, hf[q.j + 1][0], hf[q.j + 1][1], hf[q.j + 1][2], hf[q.j + 1][3], c.voffH, c.srdH, c.sofH + (q.j + 1) * 4096);
  }
  if constexpr (q.kind == 1 && (q.j & 1) && q.i == 0) wait_barv<ba_wait(q.j >> 1)>();
  if constexpr (q.kind == 2 && q.j == 15 && q.i == 0) { sm_second_all(c, std::make_integer_sequence<int, 16>{}); asm volatile("s_nop 1"); }
  if constexpr (Q < 40) __builtin_amdgcn_s_setprio(3 - ((Q + 2) & 3));
  else if constexpr (Q >= 42) { constexpr int r = (Q - 42) & 7; if constexpr (r == 0) __builtin_amdgcn_s_setprio(3); if constexpr (r == PR1) __builtin_amdgcn_s_setprio(2);
                                if constexpr (r == PR2) __builtin_amdgcn_s_setprio(1); if constexpr (r == PR3) __builtin_amdgcn_s_setprio(0); }
  asm volatile("" :: "v"(c.f[Q % 3][0]), "v"(c.f[Q % 3][1]), "v"(c.f[Q % 3][2]), "v"(c.f[Q % 3][3]));
  SBAR();
  k2_slot<Q, 0>(c, hf); k2_slot<Q, 1>(c, hf); k2_slot<Q, 2>(c, hf); k2_slot<Q, 3>(c, hf); k2_slot<Q, 4>(c, hf); k2_slot<Q, 5>(c, hf); k2_slot<Q, 6>(c, hf); k2_slot<Q, 7>(c, hf);
  if constexpr (q.kind == 2 && q.i == 1 && q.j < 15) apply_pend(c);
}
template <int Q0, int... Qs> DI void k2_quads(K2C& c, bf16x8 (&hf)[10][4], std::integer_sequence<int, Qs...>) { (k2_quad<Q0 + Qs>(c, hf), ...); }

__global__ __launch_bounds__(512, 2) void k2_attention(const u16* __restrict__ wq, const u16* __restrict__ kc, const u16* __restrict__ vc,
                                                        const u16* __restrict__ wo, const u16* __restrict__ wd, const u16* __restrict__ wg, const u16* __restrict__ wu,
                                                        const u16* __restrict__ wmod, char* __restrict__ ws) {
  extern __shared__ __attribute__((aligned(16))) char smem[];
  const int tid = threadIdx.x, lane = tid & 63, bid = blockIdx.x;
  const unsigned w = rfl(tid >> 6);
  asm volatile("adaln_code_begin:");
  const int head = ((bid & 7) >> 1) * 8 + ((bid >> 3) & 7), qb = (bid & 1) * 4 + (bid >> 6);
  STAMP_DECL; STAMP(0);
  const int hrow = qb * 256 + head * 8 + (lane & 7), hch = lane >> 3;
  const unsigned ho0 = h_off(hrow, w * 10 + hch), ho1 = lane < 16 ? h_off(hrow, w * 10 + 8 + hch) : 0x7fffffffu;
  f32x4 mp[5]; u32x2 ln; bf16x8 xv[2];
  const int ml = min(lane, 39);
  {
    const __amdgpu_buffer_rsrc_t srdM = mk_srd(ws, WS_Y);
    const unsigned vo = (ml < 20 ? w * 80 + ml * 4 : D + w * 80 + (ml - 20) * 4) * 4;
    asm volatile("s_nop 4\n\tbuffer_load_dwordx4 %0, %8, %9, 0 offen\n\tbuffer_load_dwordx4 %1, %8, %9, %10 offen\n\tbuffer_load_dwordx4 %2, %8, %9, %11 offen\n\t"
                 "buffer_load_dwordx4 %3, %8, %9, %12 offen\n\tbuffer_load_dwordx4 %4, %8, %9, %13 offen\n\tbuffer_load_dwordx2 %5, %14, %9, %15 offen\n\t"
                 "buffer_load_dwordx4 %6, %16, %9, %18 offen\n\tbuffer_load_dwordx4 %7, %17, %9, %18 offen\n\ts_barrier"
                 : "=&v"(mp[0]), "=&v"(mp[1]), "=&v"(mp[2]), "=&v"(mp[3]), "=&v"(mp[4]), "=&v"(ln), "=&v"(xv[0]), "=&v"(xv[1])
                 : "v"(vo), "s"(srdM), "s"(5120), "s"(10240), "s"(15360), "s"(20480), "v"(hrow * 8), "s"((unsigned)WS_LN), "v"(ho0), "v"(ho1), "s"((unsigned)WS_XT) : "memory");
  }
  SBAR();
  K2C c;
  const unsigned row = (lane >> 2) & 7, sub = lane >> 5, ch4 = lane & 3, sb = (unsigned)(size_t)(LAS char*)smem;
  c.srdW = mk_srd(wq, D * DQ * 2); c.voffW = row * 8192 + sub * 64 + ch4 * 16; c.m0T = sb + (w >> 1) * 4104 + (w & 1) * 2056; c.sofW = w * 8 * 8192 + head * 256;
  c.m0X = sb + L2_SIDE + 36864 + (w & 3) * 1024;
  k2_dma<0>(c); k2_dma<1>(c); k2_dma<2>(c);
  SBAR();
  c.srdK = mk_srd(kc, NH * SK * DH * 2); c.srdV = mk_srd(vc, NH * SK * DH * 2); c.srdH = mk_srd(ws + WS_H, S * D * 2);
  {
    c.voffV = row * 256 + sub * 64 + ch4 * 16;
    c.voffK = row * 256 + sub * 64 + ((ch4 ^ (w & 2)) * 16);
    c.voffH = lane * 16;
    c.m0K = sb + w * 2048;
    c.sofKV = (head * SK + (SK - W) + w * 8) * 256;
    c.sofH = (qb * 8 + w) * 40 * 1024;
    const int g = lane >> 4, q = (lane >> 2) & 3, p = lane & 3;
    c.aT = sb + (g >> 1) * 4104 + (g & 1) * 2056 + q * 64 + p * 16;
    c.aK = sb + 2048 * q + 64 * p + 16 * (g ^ (q & 2));
    const unsigned u = bid * 8 + w; const bool isO = u < 1280, live = u < 1820;
    const unsigned uu = isO ? u : u - 1280, ks = uu / 10, nb = uu - ks * 10, KSn = isO ? 128 : 54;
    c.srdSs = mk_srd(isO ? wo : wd, live ? (isO ? DQ * D * 2 : F * D * 2) : 0);
    c.srdSd = mk_srd(ws + (isO ? WS_WOT : WS_WDT), live ? (isO ? DQ * D * 2 : F * D * 2) : 0);
    c.voffS = row * (D * 2) + sub * 64 + ch4 * 16;
    c.sofSs = ks * 32 * (D * 2) + nb * 128;
#pragma unroll
    for (int i = 0; i < 4; ++i) { const unsigned C = 4 * nb + i, nt = C / 5; c.sofSd[i] = ((nt * KSn + ks) * 5 + (C - nt * 5)) * 1024; }
    {
      const unsigned m = u >= 540, r = u - m * 540, k2 = r / 27, jg = r - k2 * 27; const bool on = u < 1080;
      c.srdGs = mk_srd(m ? wu : wg, on ? D * F * 2 : 0); c.srdGd = mk_srd(ws + WS_WGU, on ? D * F * 4 : 0);
      c.voffG = row * (F * 2) + sub * 64 + ch4 * 16; c.sofGs = k2 * 32 * (F * 2) + jg * 128;
      c.voffGd = ((jg * 10 + (k2 >> 1)) * 16 + m * 4 + (k2 & 1)) * 1024 + lane * 16;
      c.pG = (const LAS char*)(size_t)(sb + L2_SIDE + w * 4096 + g * 1024 + q * 64 + p * 16);
    }
    c.srdT = mk_srd(ws + WS_WOT, DQ * D * 2); c.sofT2 = ((head & 7) * 128 + (head >> 3) * 32) * 5120 + ((bid >> 6) * 40 + w * 5) * 1024;
    {
      const unsigned sl = bid / 5, kp = bid - sl * 5, k = kp * 128 + w * 16 + 2 * (lane >> 3);
      c.srdM = mk_srd(wmod, bid < 200 ? D * 6 * D * 2 : 0); c.srdC = mk_srd(ws, WS_H);
      c.voffM = (k * 6 * D + 2 * D + sl * 64 + (lane & 7) * 8) * 2; c.voffC = WS_SC + k * 4;
      c.voffMs = (bid < 200 && lane < 8) ? WS_MOD2 + (kp * 2560 + sl * 64 + w * 8 + lane) * 4 : 0x7fffffffu;
      c.pM = (LAS f32x2*)(size_t)(sb + L2_SIDE + 32768 + (w * 2 + ((lane >> 3) & 1)) * 256 + ((lane & 7) * 8 + (lane >> 5) * 4 + ((lane >> 4) & 1) * 2) * 4);
      c.pMr = (const LAS float*)(size_t)(sb + L2_SIDE + 32768 + (lane >> 3) * 256 + (w * 8 + (lane & 7)) * 4);
    }
    c.m0S = sb + L2_SIDE + w * 4096;
    c.pS = (const LAS char*)(size_t)(c.m0S + g * 1024 + q * 64 + p * 8);
  }
  bf16x8 hf[10][4];
  {
    asm volatile("s_waitcnt vmcnt(6)" : "+v"(mp[0]), "+v"(mp[1]), "+v"(mp[2]), "+v"(mp[3]), "+v"(mp[4]), "+v"(ln), "+v"(xv[0]), "+v"(xv[1]) :: "memory");
    f32x4 mv = ((mp[0] + mp[1]) + (mp[2] + mp[3])) + mp[4];
#pragma unroll
    for (int e = 0; e < 4; ++e) { const float r = rbf(mv[e]); mv[e] = ml < 20 ? r * QSCALE : rbf(1.f + r); }
    LAS char* ab = (LAS char*)(size_t)c.m0S;
    *(LAS f32x4*)(ab + ml * 16) = mv;
    const float mean = __uint_as_float(ln[0]), rs = __uint_as_float(ln[1]) * QSCALE;
#pragma unroll
    for (int it = 0; it < 2; ++it) {
      const int ch = it ? 8 + (hch & 1) : hch;
      const f32x4 b0 = *(const LAS f32x4*)(ab + ch * 32), b1 = *(const LAS f32x4*)(ab + ch * 32 + 16), a0 = *(const LAS f32x4*)(ab + 320 + ch * 32), a1 = *(const LAS f32x4*)(ab + 336 + ch * 32);
      const u32x4 xx = __builtin_bit_cast(u32x4, xv[it]);
      u32x4 o;
      o[0] = pk2((bflo(xx[0]) - mean) * rs * a0[0] + b0[0], (bfhi(xx[0]) - mean) * rs * a0[1] + b0[1]);
      o[1] = pk2((bflo(xx[1]) - mean) * rs * a0[2] + b0[2], (bfhi(xx[1]) - mean) * rs * a0[3] + b0[3]);
      o[2] = pk2((bflo(xx[2]) - mean) * rs * a1[0] + b1[0], (bfhi(xx[2]) - mean) * rs * a1[1] + b1[1]);
      o[3] = pk2((bflo(xx[3]) - mean) * rs * a1[2] + b1[2], (bfhi(xx[3]) - mean) * rs * a1[3] + b1[3]);
      st16<16>(nopoison(o), c.srdH, it ? ho1 : ho0);
    }
  }
#pragma unroll
  for (int dt = 0; dt < 16; ++dt) { c.o[dt >> 1][dt & 1] = f32x4{}; PIN(c.o[dt >> 1][dt & 1]); }
  STAMP(1);
  asm volatile("s_barrier" ::: "memory");
  first8(hf[0], hf[1], c.voffH, c.srdH, c.sofH, c.sofH + 4096);
  asm volatile("s_barrier" ::: "memory");
  STAMP(2);
  frag_ld_all<0>(c, std::make_integer_sequence<int, 8>{}); frag_ld_all<1>(c, std::make_integer_sequence<int, 8>{});
  SBAR();
  k2_quads<0>(c, hf, std::make_integer_sequence<int, 20>{});
  STAMP(3);
  k2_quads<20>(c, hf, std::make_integer_sequence<int, 20>{});
  asm volatile("s_nop 7" ::: "memory"); SBAR();
  STAMP(4);
  q_pack_last(c, std::make_integer_sequence<int, 8>{});
  c.negm[0] = c.negm[1] = f32x4{};
  SBAR();
  asm volatile("s_nop 1" : "+v"(c.negm[0]), "+v"(c.negm[1]), "+v"(c.qf[0][0]), "+v"(c.qf[3][1]));
  k2_quad<40>(c, hf); k2_quad<41>(c, hf);
  asm volatile("s_nop 7" ::: "memory"); SBAR();
  c.aone = lane < 16 ? 1.f : 0.f;
#pragma unroll
  for (int u = 0; u < 2; ++u) {
    const f32x4 a = c.sA[0][u], b = c.sA[1][u];
    const float m = max1632(fmaxf(fmaxf(fmaxf(a[0], a[1]), fmaxf(a[2], a[3])), fmaxf(fmaxf(b[0], b[1]), fmaxf(b[2], b[3]))));
    c.sA[0][u] -= m; c.sA[1][u] -= m; c.negm[u] = (f32x4){-m, -m, -m, -m}; PIN(c.negm[u]); c.fp[u] = 1.f;
#pragma unroll
    for (int r = 0; r < 4; ++r) { c.sA[0][u][r] = EX2(c.sA[0][u][r]); if (u == 0) c.sA[1][0][r] = EX2(c.sA[1][0][r]); }
    c.l[u] = (c.sA[0][u][0] + c.sA[0][u][1]) + (c.sA[0][u][2] + c.sA[0][u][3]);
  }
  c.l[0] += c.sA[1][0][0] + c.sA[1][0][1];
  SBAR();
  STAMP(5);
  k2_quads<42>(c, hf, std::make_integer_sequence<int, 8>{});
  STAMP(6);
  k2_quads<50>(c, hf, std::make_integer_sequence<int, 24>{});
  STAMP(7);
  k2_quads<74>(c, hf, std::make_integer_sequence<int, 30>{});
  asm volatile("s_nop 7" ::: "memory"); SBAR();
  STAMP(8);
  char* ob = ws + WS_O + (size_t)((qb * 8 + w) * 128 + head * 4) * 2048 + lane * 16;
#pragma unroll
  for (int u = 0; u < 2; ++u) {
    const float il = 1.f / sum1632(c.l[u]);
#pragma unroll
    for (int s = 0; s < 4; ++s) *(bf16x8*)(ob + s * 2048 + u * 1024) = pack44(c.o[2 * s][u], c.o[2 * s + 1][u], il);
  }
  STAMP(9); STAMP_FLUSH(2, STAMP_WAVE);
}

#define W4A 2
typedef __attribute__((address_space(1))) unsigned gu32;
constexpr int RLD = 84;
constexpr int L_OWN = 131072, L_CS = L_OWN, L_W5X = L_OWN + 1024, L_MOD = L_OWN + 30720, LDS3 = L_MOD + 2048;
struct K3C {
  __amdgpu_buffer_rsrc_t srdW, srdO, srdM, srdX; unsigned voff, m0base, sofW, sofO, voffM, voffX, voffX1;
  const LAS char *pLo, *pHi; bf16x8 fw[3][5], xv[2]; f32x4 acc[2][5]; float mp[5];
};
constexpr int k3_wr(int j) { return j < 16 ? 1 : 0; }
constexpr int k3_r0(int j) { return 4 + j; }
constexpr bool k3_bar(int j) { return j == 1 || j == 4 || j == 6 || j == 9 || j == 14 || j == 20; }
constexpr int k3_ex(int j) { return j == 28 ? 7 : 0; }
constexpr int k3_ops(int j) { return k3_wr(j) + k3_ex(j) + (j + 4 < 32 ? 2 : 0); }
constexpr int k3_wait(int j) { return j == 0 ? 8 : j == 1 ? 4 + k3_ops(0) : j == 2 ? 2 + k3_ops(0) + k3_ops(1) : k3_ops(j - 3) + k3_ops(j - 2) + k3_ops(j - 1); }
template <int R> DI void k3_dw(const K3C& c) { dma16i<(R & 15) * 8192, R * 8192>(c.m0base, c.voff, c.srdW, c.sofW); }
template <int J, int R2> DI void k3_lo(const K3C& c, bf16x8& r) { ld16<R2 * 1024 + (J & 1) * 2048>(r, c.voff, c.srdO, c.sofO + (J >> 1) * 4096); }
DI bf16x8 k3_rd(const K3C& c, int piece) { const int pp = piece & 127; return lds_rd(pp < 64 ? c.pLo + pp * 1024 : c.pHi + (pp - 64) * 1024); }
DI void cnt_add(char* ws, int i) { __hip_atomic_fetch_add((gu32*)(ws + WS_CNT + i * 128), 1u, __ATOMIC_RELAXED, __HIP_MEMORY_SCOPE_AGENT); }
DI void cnt_wait(char* ws, int i, unsigned n) {
  gu32* cnt = (gu32*)(ws + WS_CNT + i * 128);
  for (unsigned spins = 0; rfl(__hip_atomic_load(cnt, __ATOMIC_RELAXED, __HIP_MEMORY_SCOPE_AGENT)) < n && spins < (1u << 22); ++spins) __builtin_amdgcn_s_sleep(1);
  asm volatile("" ::: "memory");
}
template <int J, int M> DI void k3_fill(K3C& c, bf16x8 (&of)[32][2]) {
  if constexpr ((M & 1) == 0) { if constexpr (J + 2 < 32) c.fw[(J + 2) % 3][M >> 1] = k3_rd(c, 5 * (J + 2) + (M >> 1)); }
  else if constexpr (M == 1) {
    if constexpr (k3_wr(J) >= 1) k3_dw<k3_r0(J)>(c);
    if constexpr (J == 28) asm volatile("buffer_load_dword %0, %5, %6, 0 offen\n\tbuffer_load_dword %1, %5, %6, %7 offen\n\tbuffer_load_dword %2, %5, %6, %8 offen\n\t"
                                        "buffer_load_dword %3, %5, %6, %9 offen\n\tbuffer_load_dword %4, %5, %6, %10 offen" : "=&v"(c.mp[0]), "=&v"(c.mp[1]), "=&v"(c.mp[2]), "=&v"(c.mp[3]), "=&v"(c.mp[4])
                                        : "v"(c.voffM), "s"(c.srdM), "s"(10240), "s"(20480), "s"(30720), "s"(40960) : "memory");
  } else if constexpr (M == 3) { if constexpr (k3_wr(J) == 2) k3_dw<k3_r0(J) + 1>(c); }
  else if constexpr (M == 5) { if constexpr (J + 4 < 32) k3_lo<J + 4, 0>(c, of[J + 4][0]); if constexpr (J == 28) ld16<0>(c.xv[0], c.voffX, c.srdX, 0); }
  else if constexpr (M == 7) { if constexpr (J + 4 < 32) k3_lo<J + 4, 1>(c, of[J + 4][1]); if constexpr (J == 28) ld16<0>(c.xv[1], c.voffX1, c.srdX, 0); }
}
template <int J, int M> DI void k3_slot(K3C& c, bf16x8 (&of)[32][2]) { MFMA16_ACC(c.acc[M & 1][M >> 1], c.fw[J % 3][M >> 1], of[J][M & 1]); k3_fill<J, M>(c, of); SBAR(); }
template <int J> DI void k3_step(K3C& c, bf16x8 (&of)[32][2]) {
  bf16x8 (&f)[5] = c.fw[J % 3];
  if constexpr (k3_bar(J)) asm volatile("s_waitcnt vmcnt(%7)\n\ts_barrier" : "+v"(of[J][0]), "+v"(of[J][1]), "+v"(f[0]), "+v"(f[1]), "+v"(f[2]), "+v"(f[3]), "+v"(f[4]) : "i"(k3_wait(J)) : "memory");
  else asm volatile("s_waitcnt vmcnt(%7)" : "+v"(of[J][0]), "+v"(of[J][1]), "+v"(f[0]), "+v"(f[1]), "+v"(f[2]), "+v"(f[3]), "+v"(f[4]) : "i"(k3_wait(J)) : "memory");
  k3_slot<J, 0>(c, of); k3_slot<J, 1>(c, of); k3_slot<J, 2>(c, of); k3_slot<J, 3>(c, of); k3_slot<J, 4>(c, of);
  k3_slot<J, 5>(c, of); k3_slot<J, 6>(c, of); k3_slot<J, 7>(c, of); k3_slot<J, 8>(c, of); k3_slot<J, 9>(c, of);
}
template <int... Js> DI void k3_steps(K3C& c, bf16x8 (&of)[32][2], std::integer_sequence<int, Js...>) { (k3_step<Js>(c, of), ...); }

struct K4C {
  __amdgpu_buffer_rsrc_t srdG, srdH, srdS, srdD; unsigned voffH, m0base, sofW, sofH, aW, voffSt, sofcs, sofcnt, m0x, flag, rot[7];
  const LAS char* pW; bf16x8 f[3][4], st[2], cs4, wr[20]; f32x4 o[8][2]; LAS char* csimg; char* ws; int j;
};
template <int I, int HALF> DI void k4_dma1(const K4C& c) { dma16i<((I + 2) & (NSLOT - 1)) * SLOT + HALF * 1024, I * 16384 + HALF * 1024>(c.m0base, c.voffH, c.srdG, c.sofW); }
template <int I> DI void k4_dma(const K4C& c) { k4_dma1<I, 0>(c); k4_dma1<I, 1>(c); }
template <int I, int S_> DI void k4_ldh1(const K4C& c, bf16x8& r) { ld16c<S_ * 1024>(r, c.voffH, c.srdH, c.sofH + I * 4096); }
template <int I> DI void k4_ldh(const K4C& c, bf16x8 (&hf)[4]) { k4_ldh1<I, 0>(c, hf[0]); k4_ldh1<I, 1>(c, hf[1]); k4_ldh1<I, 2>(c, hf[2]); k4_ldh1<I, 3>(c, hf[3]); }
template <int Q, int E> DI void k4_rd(K4C& c) {
  if constexpr (Q < 40) { if constexpr ((Q & 3) == 0 && E == 0) c.pW = slot_ptr(c.aW, (Q >> 2) + 2); c.f[Q % 3][E] = lds_rd(c.pW + ((Q & 3) * 4 + E) * 1024); }
}
constexpr int w5_off(int p) { return ((4 + (p >> 1)) & 7) * SLOT + (p & 1) * 1024; }
template <int P> DI void w5_dma(const K4C& c) { dma16(P < 14 ? c.m0base + w5_off(P) : c.m0x, c.voffH, c.srdD, c.rot[4 + P / 5] * 5120 + (P % 5) * 1024); }
template <int P> DI void w5_ld(K4C& c) { ld16<(P % 5 & 3) * 1024>(c.wr[P], c.voffH, c.srdD, c.rot[P / 5] * 5120 + (P % 5 >> 2) * 4096); }
constexpr int k4_wait(int i) { return i == 0 ? 4 : i == 1 ? 8 : i == 2 ? 10 : i < 7 ? 12 : i == 7 ? 10 : 13; }
template <int Q, int M> DI void k4_slot(K4C& c, bf16x8 (&hf)[10][4]) {
  constexpr int I = Q >> 2, X = (Q & 3) * 8 + M, dt = 2 * (Q & 3) + (M >> 2), u = (M >> 1) & 1, ks = M & 1;
  if constexpr (I == 0 && ks == 0) asm volatile("v_mfma_f32_16x16x32_bf16 %0, %1, %2, 0" : "=&v"(c.o[dt][u]) : "v"(c.f[Q % 3][2 * (M >> 2)]), "v"(hf[0][u]));
  else MFMA16_ACC(c.o[dt][u], c.f[Q % 3][2 * (M >> 2) + ks], hf[I][2 * ks + u]);
  SBAR();
  if constexpr ((M & 1) == 0) k4_rd<Q + 2, (M >> 1)>(c);
  if constexpr (I == 0) { if constexpr ((X & 3) == 1) k4_ldh1<2 + (X >> 4), (X >> 2) & 3>(c, hf[2 + (X >> 4)][(X >> 2) & 3]); }
  else {
    if constexpr (X < 16 && (X & 3) == 1 && I + 3 < 10) k4_ldh1<I + 3, (X >> 2)>(c, hf[I + 3][X >> 2]);
    if constexpr (X == 19 || X == 27) { if constexpr (I < 3) k4_dma1<I + 7, (X == 27)>(c); else w5_dma<2 * (I - 3) + (X == 27)>(c); }
  }
  if constexpr (I == 8 && X == 1) asm volatile("buffer_load_dword %0, off, %1, %2 sc1" : "=v"(c.flag) : "s"(c.srdS), "s"(c.sofcnt) : "memory");
  if constexpr (I == 9 && X == 1) {
    asm volatile("s_waitcnt vmcnt(12)" : "+v"(c.flag) :: "memory");
    if (__builtin_expect(rfl(c.flag) == 0u, 0)) cnt_wait(c.ws, CNT_C + c.j, 1u);
    ld16c<0>(c.cs4, c.voffH, c.srdS, c.sofcs);
  }
  if constexpr (I == 7 && (X == 3 || X == 7)) ld16c<(X == 7) * 1024>(c.st[X == 7], c.voffSt, c.srdS, WS_STAT);
  if constexpr ((I == 7 || I == 8) && (X == 17 || X == 21 || X == 25 || X == 29)) w5_ld<10 * (I - 7) + ((X - 17) >> 2)>(c);
  if constexpr ((I == 8 || I == 9) && X >= 3 && X < 15 && (X & 1)) w5_ld<10 * (I - 8) + 4 + ((X - 3) >> 1)>(c);
  if constexpr (I == 9 && X == 31) w5_dma<14>(c);
  SBAR();
}
template <int Q> DI void k4_quad(K4C& c, bf16x8 (&hf)[10][4]) {
  constexpr int I = Q >> 2;
  if constexpr ((Q & 3) == 2) {
    if constexpr (I < 9) wait_bar4v<k4_wait(I)>(hf[I + 1][0], hf[I + 1][1], hf[I + 1][2], hf[I + 1][3]); else asm volatile("s_barrier" ::: "memory");
    if constexpr (I == 8) asm volatile("" : "+v"(c.st[0]), "+v"(c.st[1]));
  }
  __builtin_amdgcn_s_setprio(3 - ((Q + 2) & 3));
  asm volatile("" :: "v"(c.f[Q % 3][0]), "v"(c.f[Q % 3][1]), "v"(c.f[Q % 3][2]), "v"(c.f[Q % 3][3]));
  SBAR();
  k4_slot<Q, 0>(c, hf); k4_slot<Q, 1>(c, hf); k4_slot<Q, 2>(c, hf); k4_slot<Q, 3>(c, hf); k4_slot<Q, 4>(c, hf); k4_slot<Q, 5>(c, hf); k4_slot<Q, 6>(c, hf); k4_slot<Q, 7>(c, hf);
}
template <int... Qs> DI void k4_quads(K4C& c, bf16x8 (&hf)[10][4], std::integer_sequence<int, Qs...>) { (k4_quad<Qs>(c, hf), ...); }
#define WR_ALL(c) "+v"(c.wr[0]), "+v"(c.wr[1]), "+v"(c.wr[2]), "+v"(c.wr[3]), "+v"(c.wr[4]), "+v"(c.wr[5]), "+v"(c.wr[6]), "+v"(c.wr[7]), "+v"(c.wr[8]), "+v"(c.wr[9]), \
                  "+v"(c.wr[10]), "+v"(c.wr[11]), "+v"(c.wr[12]), "+v"(c.wr[13]), "+v"(c.wr[14]), "+v"(c.wr[15]), "+v"(c.wr[16]), "+v"(c.wr[17]), "+v"(c.wr[18]), "+v"(c.wr[19])
template <int... Ps> DI void w5_dmas(const K4C& c, std::integer_sequence<int, Ps...>) { (w5_dma<Ps>(c), ...); }
template <int... Ps> DI void w5_lds(K4C& c, std::integer_sequence<int, Ps...>) { (w5_ld<Ps>(c), ...); }
DI void k5_la(bf16x8 (&r)[4], unsigned voff, __amdgpu_buffer_rsrc_t srd, unsigned so) {
  ld16c<0>(r[0], voff, srd, so); ld16c<1024>(r[1], voff, srd, so); ld16c<2048>(r[2], voff, srd, so); ld16c<3072>(r[3], voff, srd, so);
}

__global__ __launch_bounds__(512, 2) void k3_tail(const u16* __restrict__ wg, const u16* __restrict__ wu, char* __restrict__ ws, u16* __restrict__ out) {
  extern __shared__ __attribute__((aligned(16))) char smem[];
  LAS char* sm = (LAS char*)smem;
  const unsigned sb = (unsigned)(size_t)sm;
  const int tid = threadIdx.x, lane = tid & 63, bid = blockIdx.x;
  const unsigned w = rfl(tid >> 6);
  const int kq = (bid & 7) >> 1, rb = (bid & 1) * 4 + (bid >> 6), nt = (bid >> 3) & 7;
  STAMP_DECL; STAMP(0);
  const int frow = rb * 256 + kq * 64 + w * 8 + (lane & 7), fch = lane >> 3;
  const int g4 = lane >> 4, q16 = lane & 15;
  const int j4 = kq * 7 + nt; const bool act4 = nt < 7 && j4 < 27;
  u32x4 x1[2];
  K4C d;
  {
    K3C c;
    c.srdW = mk_srd(ws + WS_WOT, DQ * D * 2); c.srdO = mk_srd(ws + WS_O, S * DQ * 2);
    c.voff = lane * 16; c.m0base = sb + w * 1024;
    c.sofW = (nt * 128 + kq * 32) * 5120 + w * 1024; c.sofO = ((rb * 8 + w) * 128 + kq * 32) * 2048;
    asm volatile("s_nop 4" ::: "memory");
    bf16x8 of[32][2];
    k3_dw<0>(c); k3_dw<1>(c); k3_lo<0, 0>(c, of[0][0]); k3_lo<0, 1>(c, of[0][1]);
    asm volatile("s_barrier" ::: "memory");
    k3_dw<2>(c); k3_dw<3>(c); k3_lo<1, 0>(c, of[1][0]); k3_lo<1, 1>(c, of[1][1]);
    asm volatile("s_barrier" ::: "memory");
    k3_lo<2, 0>(c, of[2][0]); k3_lo<2, 1>(c, of[2][1]); k3_lo<3, 0>(c, of[3][0]); k3_lo<3, 1>(c, of[3][1]);
    c.pLo = sm + lane * 16; { unsigned a = sb + lane * 16 + 65536; PIN(a); c.pHi = (const LAS char*)(size_t)a; }
    c.srdM = mk_srd(ws + WS_MOD2, 5 * 2560 * 4); c.srdX = mk_srd(ws + WS_XT, S * D * 2);
    { const int t = min(tid, 239); c.voffM = 4 * (nt * 80 + (t < 80 ? t : t < 160 ? 2 * D + t - 80 : 3 * D + t - 160)); }
    c.voffX = h_off(frow, nt * 10 + fch); c.voffX1 = h_off(frow, nt * 10 + 8 + (fch & 1));
#pragma unroll
    for (int i = 0; i < 10; ++i) { c.acc[i / 5][i % 5] = f32x4{}; PIN(c.acc[i / 5][i % 5]); }
    asm volatile("s_waitcnt vmcnt(%2)\n\ts_barrier" : "+v"(of[0][0]), "+v"(of[0][1]) : "i"(k3_wait(0)) : "memory");
    STAMP(1);
#pragma unroll
    for (int cc = 0; cc < 10; ++cc) c.fw[cc / 5][cc % 5] = k3_rd(c, cc);
    SBAR();
    k3_steps(c, of, std::make_integer_sequence<int, 32>{});
    asm volatile("s_nop 7" ::: "memory"); SBAR();
    STAMP(2);
    const int tile = rb * 8 + nt, own = w >> 1;
    d.srdG = mk_srd(ws + WS_WGU, act4 ? D * F * 4 : 0);
    d.voffH = lane * 16; d.m0base = sb + w * 2048; d.sofW = j4 * 163840 + w * 2048;
    const __amdgpu_buffer_rsrc_t srdP = mk_srd(ws + WS_PART, 64 * 4 * 3 * 10240);
    if (own != kq) {
      const unsigned so = ((tile * 4 + own) * 3 + (kq - (kq > own))) * 10240 + (w & 1) * 5120;
      const unsigned vo = ((g4 & 1) * 2 + (q16 >> 3)) * 1280 + (g4 >> 1) * 128 + (q16 & 7) * 16;
#pragma unroll
      for (int cc = 0; cc < 5; ++cc) {
        const auto r0 = __builtin_amdgcn_permlane16_swap(pk2(c.acc[0][cc][0], c.acc[0][cc][1]), pk2(c.acc[1][cc][0], c.acc[1][cc][1]), false, false);
        const auto r1 = __builtin_amdgcn_permlane16_swap(pk2(c.acc[0][cc][2], c.acc[0][cc][3]), pk2(c.acc[1][cc][2], c.acc[1][cc][3]), false, false);
        st16<16>((u32x4){r0[0], r1[0], r0[1], r1[1]}, srdP, vo + so + cc * 256);
      }
      asm volatile("s_waitcnt vmcnt(0)" ::: "memory");
      if (lane == 0) cnt_add(ws, CNT_P + tile * 4 + own);
    } else {
#pragma unroll
      for (int cc = 0; cc < 5; ++cc)
#pragma unroll
        for (int r2 = 0; r2 < 2; ++r2) *(LAS f32x4*)(sm + L_OWN + (((w & 1) * 32 + r2 * 16 + q16) * RLD + cc * 16 + g4 * 4) * 4) = c.acc[r2][cc];
      asm volatile("s_waitcnt vmcnt(0)" ::: "memory");
    }
    k4_dma<0>(d);
    k4_dma<1>(d);
    asm volatile("" : "+v"(c.mp[0]), "+v"(c.mp[1]), "+v"(c.mp[2]), "+v"(c.mp[3]), "+v"(c.mp[4]), "+v"(c.xv[0]), "+v"(c.xv[1]));
    { const float mv = rbf(((c.mp[0] + c.mp[1]) + (c.mp[2] + c.mp[3])) + c.mp[4]); *(LAS float*)(sm + L_MOD + tid * 4) = (tid >= 80 && tid < 160) ? rbf(1.f + mv) : mv; }
    STAMP(3);
    if (w == 2 * kq) cnt_wait(ws, CNT_P + tile * 4 + kq, 6u);
    asm volatile("s_waitcnt lgkmcnt(0)\n\ts_barrier" ::: "memory");
    STAMP(4);
    u32x4 pv[2][3];
#pragma unroll
    for (int sI = 0; sI < 3; ++sI) {
      const unsigned so = ((tile * 4 + kq) * 3 + sI) * 10240 + w * 1280;
      pv[0][sI] = __builtin_amdgcn_raw_buffer_load_b128(srdP, c.voff, so, 16);
      pv[1][sI] = __builtin_amdgcn_raw_buffer_load_b128(srdP, 1024 + (lane & 15) * 16, so, 16);
    }
    const __amdgpu_buffer_rsrc_t srdY = mk_srd(ws + WS_Y, S * D * 2);
    float v[2][8];
#pragma unroll
    for (int it = 0; it < 2; ++it) {
      const int ch = it ? 8 + (fch & 1) : fch;
      const LAS float* po = (const LAS float*)(sm + L_OWN) + (w * 8 + (lane & 7)) * RLD + ch * 8;
      const LAS float* pm = (const LAS float*)(sm + L_MOD) + ch * 8;
      f32x4 t[2] = {*(const LAS f32x4*)po, *(const LAS f32x4*)(po + 4)};
      const f32x4 ga[2] = {*(const LAS f32x4*)pm, *(const LAS f32x4*)(pm + 4)}, a1[2] = {*(const LAS f32x4*)(pm + 80), *(const LAS f32x4*)(pm + 84)};
      const u32x4 xx = __builtin_bit_cast(u32x4, c.xv[it]);
      u32x4 y;
#pragma unroll
      for (int e = 0; e < 4; ++e) {
        const float lo = t[e >> 1][2 * (e & 1)] + ((bflo(pv[it][0][e]) + bflo(pv[it][1][e])) + bflo(pv[it][2][e]));
        const float hi = t[e >> 1][2 * (e & 1) + 1] + ((bfhi(pv[it][0][e]) + bfhi(pv[it][1][e])) + bfhi(pv[it][2][e]));
        x1[it][e] = pk2(bflo(xx[e]) + ga[e >> 1][2 * (e & 1)] * lo, bfhi(xx[e]) + ga[e >> 1][2 * (e & 1) + 1] * hi);
        v[it][2 * e] = bflo(x1[it][e]); v[it][2 * e + 1] = bfhi(x1[it][e]);
        y[e] = pk2(v[it][2 * e] * a1[e >> 1][2 * (e & 1)], v[it][2 * e + 1] * a1[e >> 1][2 * (e & 1) + 1]);
      }
      st16<16>(y, srdY, it == 0 ? c.voffX : lane < 16 ? c.voffX1 : 0x7fffffffu);
    }
    {
      float s0 = 0.f, s1 = 0.f;
#pragma unroll
      for (int e = 0; e < 8; ++e) { s0 += v[0][e]; s1 += v[1][e]; }
      const float mu = sum816(lane < 16 ? s0 + s1 : s0) * (1.f / 80.f);
      float q0 = 0.f, q1 = 0.f;
#pragma unroll
      for (int e = 0; e < 8; ++e) { q0 += (v[0][e] - mu) * (v[0][e] - mu); q1 += (v[1][e] - mu) * (v[1][e] - mu); }
      const float m2 = sum816(lane < 16 ? q0 + q1 : q0);
      d.srdS = mk_srd(ws, WS_H);
      __builtin_amdgcn_raw_buffer_store_b64((u32x2){__float_as_uint(mu), __float_as_uint(m2)}, d.srdS, lane < 8 ? frow * 64 + nt * 8 : 0x7fffffffu, WS_STAT, 16);
    }
    STAMP(5);
    asm volatile("s_waitcnt vmcnt(0)\n\ts_barrier" ::: "memory");
    if (tid < 32) cnt_add(ws, CNT_Y + rb * 32 + tid);
    STAMP(6);
  }
  {
    const int k0 = w < 6 ? 7 * w : 42 + 6 * (w - 6), nk = w < 6 ? 7 : 6;
    d.srdD = mk_srd(ws + WS_WDT + (size_t)(nt * 54 + k0) * 5120, nk * 5120);
    d.m0x = sb + L_W5X + w * 4096;
#pragma unroll
    for (int i = 0; i < 7; ++i) { const int r = i + (nt == 7 ? 3 : nt); d.rot[i] = r >= 7 ? r - 7 : r; }
  }
  if (act4) {
    k4_dma<2>(d); k4_dma<3>(d); k4_dma<4>(d); k4_dma<5>(d); k4_dma<6>(d); k4_dma<7>(d);
    d.srdH = mk_srd(ws + WS_Y, S * D * 2); d.sofH = (rb * 8 + w) * 40 * 1024;
    d.aW = sb + lane * 16;
    d.sofcs = WS_CSBS + j4 * 1024; d.sofcnt = WS_CNT + (CNT_C + j4) * 128; d.voffSt = (rb * 256 + w * 32 + q16) * 64 + g4 * 16; d.csimg = sm + L_CS + w * 4096 + lane * 16; d.ws = ws; d.j = j4;
    if (w == 0) cnt_wait(ws, CNT_Y + rb * 32 + kq * 8 + nt, 32u);
    asm volatile("s_barrier" ::: "memory");
    STAMP(7);
    bf16x8 hf[10][4];
    k4_ldh<0>(d, hf[0]); k4_ldh<1>(d, hf[1]);
    wait_bar4v<4>(hf[0][0], hf[0][1], hf[0][2], hf[0][3]);
    STAMP(8);
    k4_rd<0, 0>(d); k4_rd<0, 1>(d); k4_rd<0, 2>(d); k4_rd<0, 3>(d); k4_rd<1, 0>(d); k4_rd<1, 1>(d); k4_rd<1, 2>(d); k4_rd<1, 3>(d);
    SBAR();
    k4_quads(d, hf, std::make_integer_sequence<int, 40>{});
    __builtin_amdgcn_s_setprio(0);
    STAMP(9);
    asm volatile("s_waitcnt vmcnt(9)" : "+v"(d.cs4) :: "memory"); *(LAS bf16x8*)d.csimg = d.cs4;
    float nmr[2], rg[2], ru[2];
#pragma unroll
    for (int u = 0; u < 2; ++u) {
      const f32x4 sv = __builtin_bit_cast(f32x4, d.st[u]);
      const float mean = sum1632(sv[0] + sv[2]) * 0.125f;
      const float m2 = sum1632(sv[1] + sv[3] + 80.f * ((sv[0] - mean) * (sv[0] - mean) + (sv[2] - mean) * (sv[2] - mean)));
      const float rs = rsqrtf(m2 * (1.f / D) + EPS);
      nmr[u] = -mean * rs; rg[u] = rs * -1.4426950408889634f; ru[u] = rs * -0.6931471805599453f;
    }
    asm volatile("s_nop 3" : "+v"(nmr[0]), "+v"(nmr[1])); SBAR();
    const LAS float* cb = (const LAS float*)(sm + L_CS + w * 4096) + g4 * 8;
    const __amdgpu_buffer_rsrc_t srdA = mk_srd(ws + WS_ACT, S * F * 2);
    const unsigned vo = (((rb * 4 + (w >> 1)) * 54 + j4 * 2) * 4 + (w & 1) * 2) * 1024 + lane * 16;
#pragma unroll
    for (int s = 0; s < 2; ++s) {
      u32x4 o[2];
#pragma unroll
      for (int t = 0; t < 2; ++t) {
        const f32x4 csg = *(const LAS f32x4*)(cb + 32 * s + 4 * t), bsg = *(const LAS f32x4*)(cb + 64 + 32 * s + 4 * t);
        const f32x4 csu = *(const LAS f32x4*)(cb + 128 + 32 * s + 4 * t), bsu = *(const LAS f32x4*)(cb + 192 + 32 * s + 4 * t);
#pragma unroll
        for (int u = 0; u < 2; ++u)
#pragma unroll
          for (int e = 0; e < 2; ++e) {
            const f32x2 ag = {d.o[4 * s + t][u][2 * e], d.o[4 * s + t][u][2 * e + 1]}, au = {d.o[4 * s + 2 + t][u][2 * e], d.o[4 * s + 2 + t][u][2 * e + 1]};
            const f32x2 g = ag * rg[u] + ((f32x2){csg[2 * e], csg[2 * e + 1]} * nmr[u] + (f32x2){bsg[2 * e], bsg[2 * e + 1]});
            const f32x2 uu = au * ru[u] + ((f32x2){csu[2 * e], csu[2 * e + 1]} * nmr[u] + (f32x2){bsu[2 * e], bsu[2 * e + 1]});
            const f32x2 den = (f32x2){EX2(g[0]), EX2(g[1])} + 1.f;
            const f32x2 a = (g * uu) * (f32x2){__builtin_amdgcn_rcpf(den[0]), __builtin_amdgcn_rcpf(den[1])};
            o[u][2 * t + e] = pk2(a[0], a[1]);
          }
      }
      st16<16>(o[0], srdA, vo + s * 4096); st16<16>(o[1], srdA, vo + s * 4096 + 1024);
    }
    STAMP(10);
    asm volatile("s_waitcnt vmcnt(0)\n\ts_barrier" : WR_ALL(d) :: "memory");
    if (tid < 32) cnt_add(ws, CNT_A + rb * 32 + tid);
  } else {
    const int jc = (bid >> 6) * 8 + (bid & 7);
    asm volatile("s_waitcnt vmcnt(0)\n\ts_barrier" ::: "memory");
    if (nt == 7 && jc < 27) {
      const float* mod = (const float*)(ws + WS_MOD2) + D;
      const int ch = tid & 15, rg = tid >> 4;
      float mv[3];
#pragma unroll
      for (int i = 0; i < 3; ++i) { const int t = min(tid + 512 * i, 1279); mv[i] = ((mod[t] + mod[2560 + t]) + (mod[5120 + t] + mod[7680 + t])) + mod[10240 + t]; }
      const u16* wsrc = ((ch & 8) ? wu : wg) + jc * 64 + (ch & 7) * 8;
      u32x4 wv[20];
#pragma unroll
      for (int i = 0; i < 20; ++i) wv[i] = *(const u32x4*)(wsrc + (size_t)(rg + 32 * i) * F);
      LAS float* mm = (LAS float*)sm;
#pragma unroll
      for (int i = 0; i < 3; ++i) { const int t = min(tid + 512 * i, 1279); const float r = rbf(mv[i]); mm[t] = t < D ? r : rbf(1.f + r); }
      asm volatile("s_waitcnt lgkmcnt(0)\n\ts_barrier" ::: "memory");
      float ac[8] = {0, 0, 0, 0, 0, 0, 0, 0}, ab[8] = {0, 0, 0, 0, 0, 0, 0, 0};
#pragma unroll
      for (int i = 0; i < 20; ++i) {
        const float sbv = mm[rg + 32 * i], a = mm[D + rg + 32 * i];
#pragma unroll
        for (int e = 0; e < 4; ++e) { const float lo = bflo(wv[i][e]), hi = bfhi(wv[i][e]); ac[2 * e] += a * lo; ac[2 * e + 1] += a * hi; ab[2 * e] += sbv * lo; ab[2 * e + 1] += sbv * hi; }
      }
#pragma unroll
      for (int e = 0; e < 8; ++e) { ac[e] = sum1632(ac[e]); ab[e] = sum1632(ab[e]); }
      LAS float* rd = mm + 2048 + w * 256 + (ch >> 3) * 128 + (ch & 7) * 8;
      if (lane < 16) { *(LAS f32x4*)rd = (f32x4){ac[0], ac[1], ac[2], ac[3]}; *(LAS f32x4*)(rd + 4) = (f32x4){ac[4], ac[5], ac[6], ac[7]};
                       *(LAS f32x4*)(rd + 64) = (f32x4){ab[0], ab[1], ab[2], ab[3]}; *(LAS f32x4*)(rd + 68) = (f32x4){ab[4], ab[5], ab[6], ab[7]}; }
      asm volatile("s_waitcnt lgkmcnt(0)\n\ts_barrier" ::: "memory");
      if (tid < 256) {
        const LAS float* r = mm + 2048 + tid;
        const float t = (((r[0] + r[256]) + (r[512] + r[768])) + ((r[1024] + r[1280]) + (r[1536] + r[1792]))) * (tid < 128 ? -1.4426950408889634f : -0.6931471805599453f);
        __builtin_amdgcn_raw_buffer_store_b32(__float_as_uint(t), d.srdS, tid * 4, WS_CSBS + jc * 1024, 16);
      }
      asm volatile("s_waitcnt vmcnt(0) lgkmcnt(0)\n\ts_barrier" ::: "memory");
      if (tid == 0) cnt_add(ws, CNT_C + jc);
    }
    w5_dmas(d, std::make_integer_sequence<int, 15>{}); w5_lds(d, std::make_integer_sequence<int, 20>{});
    asm volatile("s_waitcnt vmcnt(0)" : WR_ALL(d) :: "memory");
  }
  STAMP(11);
  {
    const int k0 = w < 6 ? 7 * w : 42 + 6 * (w - 6), nk = w < 6 ? 7 : 6;
    const __amdgpu_buffer_rsrc_t srdA = mk_srd(ws + WS_ACT + (size_t)((rb * 4 + kq) * 54 + k0) * 4096, nk * 4096);
    f32x4 gb[2][2];
#pragma unroll
    for (int it = 0; it < 2; ++it) { const LAS float* pm = (const LAS float*)(sm + L_MOD) + 160 + (it ? 8 + (fch & 1) : fch) * 8; gb[it][0] = *(const LAS f32x4*)pm; gb[it][1] = *(const LAS f32x4*)(pm + 4); }
    if (w == 0) cnt_wait(ws, CNT_A + rb * 32 + kq * 8 + nt, 27u);
    asm volatile("s_waitcnt lgkmcnt(0)\n\ts_barrier\n\ts_nop 4" : "+v"(gb[0][0]), "+v"(gb[0][1]), "+v"(gb[1][0]), "+v"(gb[1][1]) :: "memory");
    STAMP(12);
    bf16x8 fa[7][4];
    k5_la(fa[0], d.voffH, srdA, d.rot[0] * 4096);
    asm volatile("s_barrier" ::: "memory");
    k5_la(fa[1], d.voffH, srdA, d.rot[1] * 4096); k5_la(fa[2], d.voffH, srdA, d.rot[2] * 4096);
    f32x4 acc[4][5] = {};
    const LAS char* pw = sm + w * 2048 + lane * 16; const LAS char* pwh; { unsigned a = sb + w * 2048 + lane * 16 + 65536; PIN(a); pwh = (const LAS char*)(size_t)a; }
    const LAS char* pwx = sm + L_W5X + w * 4096 + lane * 16;
    bf16x8 fl[5];
#define K5_STEP(J) do { \
      asm volatile("s_waitcnt vmcnt(%4)" : "+v"(fa[J][0]), "+v"(fa[J][1]), "+v"(fa[J][2]), "+v"(fa[J][3]) : "i"(J < 5 ? 8 : J == 5 ? 4 : 0) : "memory"); \
      _Pragma("unroll") for (int cc = 0; cc < 5; ++cc) { \
        _Pragma("unroll") for (int r = 0; r < 4; ++r) acc[r][cc] = MFMA16(J < 4 ? d.wr[(J < 4 ? J : 0) * 5 + cc] : fl[cc], fa[J][r], acc[r][cc]); \
        if (J >= 3 && J < 6) { const int pp = (J - 3) * 5 + cc; fl[cc] = lds_rd(pp == 14 ? pwx : w5_off(pp) < 65536 ? pw + w5_off(pp) : pwh + (w5_off(pp) - 65536)); } \
        SBAR(); } \
      if (J + 3 < 7) k5_la(fa[J + 3 < 7 ? J + 3 : 0], d.voffH, srdA, d.rot[J + 3 < 7 ? J + 3 : 0] * 4096); \
      SBAR(); } while (0)
    K5_STEP(0); K5_STEP(1); K5_STEP(2); K5_STEP(3); K5_STEP(4); K5_STEP(5); K5_STEP(6);
    STAMP(13);
    {
      const unsigned lo = q16 * 32 + (((g4 & 1) ^ ((q16 >> 2) & 1)) * 16);
      unsigned a0 = sb + w * 2048 + (g4 >> 1) * SLOT + lo, a1 = a0 + 65536, a2 = sb + L_OWN + w * 4096 + (g4 >> 1) * 2048 + lo;
      PIN(a0); PIN(a1); PIN(a2);
#pragma unroll
      for (int cc = 0; cc < 5; ++cc)
#pragma unroll
        for (int r = 0; r < 4; ++r) *(LAS f32x4*)((LAS char*)(size_t)((cc < 2 ? a0 : cc < 4 ? a1 : a2) ^ ((cc & 1) * 16)) + (cc & 1) * 2 * SLOT + r * 512) = acc[r][cc];
    }
    asm volatile("s_waitcnt lgkmcnt(0)\n\ts_barrier" ::: "memory");
    STAMP(14);
    const __amdgpu_buffer_rsrc_t srdOut = mk_srd(out, S * D * 2);
#pragma unroll
    for (int it = 0; it < 2; ++it) {
      if (it == 1 && lane >= 16) break;
      const int ch = it ? 8 + fch : fch, r = w * 8 + (lane & 7), sw = (((r >> 2) ^ (ch >> 1)) & 1) * 16;
      const LAS char* pr = sm + (it ? L_OWN + (fch & 1) * 2048 : fch * SLOT) + r * 32;
      f32x4 s0 = {}, s1 = {};
#pragma unroll
      for (int i = 0; i < 8; ++i) { s0 += *(const LAS f32x4*)(pr + i * (it ? 4096 : 2048) + sw); s1 += *(const LAS f32x4*)(pr + i * (it ? 4096 : 2048) + (sw ^ 16)); }
      const f32x4 g0 = gb[it][0], g1 = gb[it][1];
      u32x4 o;
      o[0] = pk2(bflo(x1[it][0]) + g0[0] * s0[0], bfhi(x1[it][0]) + g0[1] * s0[1]); o[1] = pk2(bflo(x1[it][1]) + g0[2] * s0[2], bfhi(x1[it][1]) + g0[3] * s0[3]);
      o[2] = pk2(bflo(x1[it][2]) + g1[0] * s1[0], bfhi(x1[it][2]) + g1[1] * s1[1]); o[3] = pk2(bflo(x1[it][3]) + g1[2] * s1[2], bfhi(x1[it][3]) + g1[3] * s1[3]);
      st16<0>(o, srdOut, (frow * D + nt * 80 + ch * 8) * 2);
    }
  }
  STAMP(15); STAMP_FLUSH(act4 ? 3 : 4, STAMP_WAVE);
  asm volatile("adaln_code_end:");
}

extern "C" void kernel_launch(void* const* d_in, const int* in_sizes, int n_in, void* d_out, int out_size, void* d_ws, size_t ws_size, hipStream_t stream) {
  const u16* x = (const u16*)d_in[0]; const u16* kc = (const u16*)d_in[1]; const u16* vc = (const u16*)d_in[2];
  const u16* wq = (const u16*)d_in[3]; const u16* wo = (const u16*)d_in[4];
  const u16* wg = (const u16*)d_in[6]; const u16* wu = (const u16*)d_in[7]; const u16* wd = (const u16*)d_in[8];
  const u16* c = (const u16*)d_in[9]; const u16* wmod = (const u16*)d_in[10];
  char* ws = (char*)d_ws + (size_t)WS_BASE * 1048576; u16* out = (u16*)d_out;
  constexpr int LDS2 = L2_SIDE + 40960;
  (void)hipFuncSetAttribute((const void*)k2_attention, hipFuncAttributeMaxDynamicSharedMemorySize, LDS2);
  (void)hipFuncSetAttribute((const void*)k3_tail, hipFuncAttributeMaxDynamicSharedMemorySize, LDS3);
  hipLaunchKernelGGL(k1_front, dim3(K1_LN + K1_MOD), dim3(256), 0, stream, x, c, wmod, wq, ws);
  hipLaunchKernelGGL(k2_attention, dim3(256), dim3(512), LDS2, stream, wq, kc, vc, wo, wd, wg, wu, wmod, ws);
  hipLaunchKernelGGL(k3_tail, dim3(256), dim3(512), LDS3, stream, wg, wu, ws, out);
}
```

```cpp
#include <hip/hip_runtime.h>
#include <hip/hip_bf16.h>
#include <stdint.h>
#include <utility>

typedef __bf16   bf16x8 __attribute__((ext_vector_type(8)));
typedef __bf16   bf16x2 __attribute__((ext_vector_type(2)));
typedef short    s16x4  __attribute__((ext_vector_type(4)));
typedef float    f32x4  __attribute__((ext_vector_type(4)));
typedef float    f32x2  __attribute__((ext_vector_type(2)));
typedef unsigned u32x4  __attribute__((ext_vector_type(4)));
typedef unsigned u32x2  __attribute__((ext_vector_type(2)));
typedef unsigned short u16;
#define LAS __attribute__((address_space(3)))
#define DI __device__ __forceinline__
#define SBAR() __builtin_amdgcn_sched_barrier(0)
#define MFMA16(a, b, c) __builtin_amdgcn_mfma_f32_16x16x32_bf16((a), (b), (c), 0, 0, 0)
#define MFMA16_ACC(c, a, b) asm volatile("v_mfma_f32_16x16x32_bf16 %0, %1, %2, %0" : "+v"(c) : "v"(a), "v"(b))


constexpr int S = 2048, D = 640, NH = 32, DH = 128, SK = 2048, W = 512, F = 1728, DQ = NH * DH;
constexpr float EPS = 1e-5f;

constexpr size_t WS_MOD  = 0;
constexpr size_t WS_EP   = 32768;
constexpr size_t WS_CSBS = 49152;
constexpr size_t WS_CNT  = 393216;
constexpr size_t WS_TAG  = 131072;
constexpr size_t WS_MOD2 = 327680;
constexpr size_t WS_H    = 589824;
constexpr size_t WS_O    = WS_H + (size_t)S * D * 2;
constexpr size_t WS_ACT  = WS_O + (size_t)S * DQ * 2;
constexpr size_t WS_WOT  = WS_ACT + (size_t)S * F * 2;
constexpr size_t WS_WDT  = WS_WOT + (size_t)DQ * D * 2;
constexpr size_t WS_WGU  = WS_WDT + (size_t)F * D * 2;
constexpr size_t WS_PART = WS_WGU + (size_t)F * D * 4;
constexpr unsigned Z_Y = 64 * 4 * 3 * 10240, Z_STAT = Z_Y + S * D * 2, Z_END = Z_STAT + S * 64;
static_assert(Z_END - Z_Y <= 2048 * 2 * 1024, "two stores per wave mark y and the statistics unwritten");
constexpr int CNT_P = 0, CNT_A = 512, CNT_O = 768, CNT_C = 1024, CNT_SA = 1056, CNT_SB = 1064;
static_assert(WS_CSBS + 27 * 1024 <= WS_TAG && WS_MOD2 + 5 * 2560 * 4 <= WS_CNT && WS_CNT + 1280 * 128 <= WS_H, "ws map");

DI float bflo(unsigned v) { return __uint_as_float(v << 16); }
DI float bfhi(unsigned v) { return __uint_as_float(v & 0xffff0000u); }
DI unsigned pk2(float lo, float hi) { f32x2 v = {lo, hi}; bf16x2 b = __builtin_convertvector(v, bf16x2); return __builtin_bit_cast(unsigned, b); }
DI float rbf(float x) { return bflo(pk2(x, 0.f)); }
DI unsigned h_off(int row, int cg) { return (((row >> 5) * 20 + (cg >> 2)) * 2 + ((row >> 4) & 1)) * 1024 + ((row & 15) + 16 * (cg & 3)) * 16; }
DI unsigned rfl(unsigned x) { return __builtin_amdgcn_readfirstlane(x); }

template <int MO, int SO, int NT = 0> DI void dma16i(unsigned m0b, unsigned voff, __amdgpu_buffer_rsrc_t srd, unsigned sob) {
  unsigned t;
  if constexpr (NT == 2) asm volatile("s_add_u32 m0, %1, %5\n\ts_add_u32 %0, %4, %6\n\tbuffer_load_dwordx4 %2, %3, %0 offen sc1 lds" : "=&s"(t) : "s"(m0b), "v"(voff), "s"(srd), "s"(sob), "i"(MO), "i"(SO) : "memory", "scc");
  else if constexpr (NT == 1) asm volatile("s_add_u32 m0, %1, %5\n\ts_add_u32 %0, %4, %6\n\tbuffer_load_dwordx4 %2, %3, %0 offen nt lds" : "=&s"(t) : "s"(m0b), "v"(voff), "s"(srd), "s"(sob), "i"(MO), "i"(SO) : "memory", "scc");
  else
  asm volatile("s_add_u32 m0, %1, %5\n\ts_add_u32 %0, %4, %6\n\tbuffer_load_dwordx4 %2, %3, %0 offen lds" : "=&s"(t) : "s"(m0b), "v"(voff), "s"(srd), "s"(sob), "i"(MO), "i"(SO) : "memory", "scc");
}
template <int OFF> DI void ld16(bf16x8& r, unsigned voff, __amdgpu_buffer_rsrc_t srd, unsigned soff) {
  asm volatile("buffer_load_dwordx4 %0, %1, %2, %3 offen offset:%4" : "=v"(r) : "v"(voff), "s"(srd), "s"(soff), "i"(OFF) : "memory");
}
template <int OFF> DI void ld16c(bf16x8& r, unsigned voff, __amdgpu_buffer_rsrc_t srd, unsigned soff) {
  asm volatile("buffer_load_dwordx4 %0, %1, %2, %3 offen offset:%4 sc1" : "=v"(r) : "v"(voff), "s"(srd), "s"(soff), "i"(OFF) : "memory");
}
template <int AUX> DI void st16(u32x4 v, __amdgpu_buffer_rsrc_t srd, unsigned voff) { __builtin_amdgcn_raw_buffer_store_b128(v, srd, voff, 0, AUX); }
DI unsigned umax4(const u32x4& v, unsigned m) { return max(max(max(m, v[0]), v[1]), max(v[2], v[3])); }
DI unsigned umax4(const bf16x8& v, unsigned m) { return umax4(__builtin_bit_cast(u32x4, v), m); }
DI bool pending(unsigned m) { return __builtin_expect(__any(m == 0xffffffffu), 0); }
DI u32x4 nopoison(u32x4 v) { return (u32x4){min(v[0], 0xfffffffeu), min(v[1], 0xfffffffeu), min(v[2], 0xfffffffeu), min(v[3], 0xfffffffeu)}; }
constexpr unsigned SPIN_CAP = 1u << 16;
DI unsigned umax16(const bf16x8& a, const bf16x8& b, const bf16x8& c, const bf16x8& d) {
  const u32x4 p = __builtin_bit_cast(u32x4, a), q = __builtin_bit_cast(u32x4, b), r = __builtin_bit_cast(u32x4, c), t = __builtin_bit_cast(u32x4, d);
  auto m3 = [](unsigned x, unsigned y, unsigned z) { return max(max(x, y), z); };
  unsigned m = m3(p[0], p[1], p[2]), n = m3(p[3], q[0], q[1]);
  m = m3(m, q[2], q[3]); n = m3(n, r[0], r[1]); m = m3(m, r[2], r[3]); n = m3(n, t[0], t[1]); m = m3(m, t[2], t[3]);
  return max(m, n);
}
template <int STEP> DI void reload4(unsigned mx, bf16x8& a, bf16x8& b, bf16x8& c, bf16x8& d, unsigned voff, __amdgpu_buffer_rsrc_t srd, unsigned soff) {
  if (pending(mx)) {
    unsigned spins = 0;
    do asm volatile("buffer_load_dwordx4 %0, %4, %5, %6 offen sc1\n\tbuffer_load_dwordx4 %1, %4, %5, %6 offen offset:%7 sc1\n\t"
                    "buffer_load_dwordx4 %2, %4, %5, %6 offen offset:%8 sc1\n\tbuffer_load_dwordx4 %3, %4, %5, %6 offen offset:%9 sc1\n\ts_waitcnt vmcnt(0)"
                    : "+v"(a), "+v"(b), "+v"(c), "+v"(d) : "v"(voff), "s"(srd), "s"(soff), "i"(STEP), "i"(2 * STEP), "i"(3 * STEP) : "memory");
    while (__any(umax16(a, b, c, d) == 0xffffffffu) && ++spins < SPIN_CAP);
  }
}
DI bf16x8 lds_rd(const LAS char* p) { return *(const LAS bf16x8*)p; }
DI bf16x8 lds_tr(const LAS char* p0, const LAS char* p1) {
  s16x4 lo = __builtin_amdgcn_ds_read_tr16_b64_v4i16((LAS s16x4*)p0);
  s16x4 hi = __builtin_amdgcn_ds_read_tr16_b64_v4i16((LAS s16x4*)p1);
  return __builtin_bit_cast(bf16x8, __builtin_shufflevector(lo, hi, 0, 1, 2, 3, 4, 5, 6, 7));
}
DI __amdgpu_buffer_rsrc_t mk_srd(const void* p, unsigned bytes) { return __builtin_amdgcn_make_buffer_rsrc((void*)p, (short)0, (int)bytes, 0x00020000); }

constexpr int SLOT = 16384, NSLOT = 8;
constexpr float QSCALE = 0.08838834764831845f * 1.4426950408889634f;
constexpr float RTHR = 64.f;

#define PIN(x) asm volatile("" : "+v"(x))
#define EX2(x) __builtin_amdgcn_exp2f(x)
template <int N> DI void wait_bar4v(bf16x8& a, bf16x8& b, bf16x8& c, bf16x8& d) {
  asm volatile("s_waitcnt vmcnt(%4)\n\ts_barrier" : "+v"(a), "+v"(b), "+v"(c), "+v"(d) : "i"(N) : "memory");
}
template <int N> DI void wait_barv() { asm volatile("s_waitcnt vmcnt(%0)\n\ts_barrier" :: "i"(N) : "memory"); }
DI const LAS char* slot_ptr(unsigned lanebase, int tile) { unsigned a = lanebase + (tile & (NSLOT - 1)) * SLOT; PIN(a); return (const LAS char*)(size_t)a; }
DI float sum1632(float v) {
  { auto r = __builtin_amdgcn_permlane16_swap(__float_as_uint(v), __float_as_uint(v), false, false); v = __uint_as_float(r[0]) + __uint_as_float(r[1]); }
  { auto r = __builtin_amdgcn_permlane32_swap(__float_as_uint(v), __float_as_uint(v), false, false); v = __uint_as_float(r[0]) + __uint_as_float(r[1]); }
  return v;
}
DI float xor8(float v) { return __int_as_float(__builtin_amdgcn_update_dpp(0, __float_as_int(v), 0x128, 0xf, 0xf, true)); }
DI float sum816(float v) { return sum1632(v + xor8(v)); }
typedef __attribute__((address_space(1))) unsigned gu32;
DI void cnt_add(char* ws, int i) { __hip_atomic_fetch_add((gu32*)(ws + WS_CNT + i * 128), 1u, __ATOMIC_RELAXED, __HIP_MEMORY_SCOPE_AGENT); }
DI void cnt_wait(char* ws, int i, unsigned n) {
  gu32* cnt = (gu32*)(ws + WS_CNT + i * 128);
  for (unsigned spins = 0; rfl(__hip_atomic_load(cnt, __ATOMIC_RELAXED, __HIP_MEMORY_SCOPE_AGENT)) < n && spins < (1u << 18); ++spins) __builtin_amdgcn_s_sleep(1);
  asm volatile("" ::: "memory");
}
DI void shards_wait(unsigned v, char* ws, int i, int lane) {
  if (__builtin_expect(__any(v < 32u), 0)) {
    gu32* cnt = (gu32*)(ws + WS_CNT + (i + (lane & 7)) * 128);
    for (unsigned spins = 0; __any(__hip_atomic_load(cnt, __ATOMIC_RELAXED, __HIP_MEMORY_SCOPE_AGENT) < 32u) && spins < (1u << 18); ++spins) __builtin_amdgcn_s_sleep(8);
  }
  asm volatile("" ::: "memory");
}
DI float sum64(float v) {
  v += __int_as_float(__builtin_amdgcn_update_dpp(0, __float_as_int(v), 0xb1, 0xf, 0xf, true));
  v += __int_as_float(__builtin_amdgcn_update_dpp(0, __float_as_int(v), 0x4e, 0xf, 0xf, true));
  v += __int_as_float(__builtin_amdgcn_update_dpp(0, __float_as_int(v), 0x124, 0xf, 0xf, true));
  return sum816(v);
}
DI float max1632(float v) {
  { auto r = __builtin_amdgcn_permlane16_swap(__float_as_uint(v), __float_as_uint(v), false, false); v = __builtin_fmaxf(__uint_as_float(r[0]), __uint_as_float(r[1])); }
  { auto r = __builtin_amdgcn_permlane32_swap(__float_as_uint(v), __float_as_uint(v), false, false); v = __builtin_fmaxf(__uint_as_float(r[0]), __uint_as_float(r[1])); }
  return v;
}
DI bf16x8 pack44(const f32x4& a, const f32x4& b, float m) { return __builtin_bit_cast(bf16x8, (u32x4){pk2(a[0] * m, a[1] * m), pk2(a[2] * m, a[3] * m), pk2(b[0] * m, b[1] * m), pk2(b[2] * m, b[3] * m)}); }

constexpr int S2 = 16416, NS2 = 7, L2_SIDE = NS2 * S2;
struct K2C {
  __amdgpu_buffer_rsrc_t srdW, srdK, srdV, srdH;
  unsigned voffW, voffK, voffV, voffH, m0T, m0K, sofW, sofKV, sofH, aT, aK;
  const LAS char *pT, *pK;
  bf16x8 f[3][4];
  bf16x8 qf[4][2];
  f32x4 o[8][2], sA[2][2], sB[2][2], negm[2];
  u32x4 pA[2], pB[2];
  f32x4 ls[2]; u32x4 ones; float fp[2], aone; int pend, mx[2]; unsigned chk;
  __amdgpu_buffer_rsrc_t srdSs, srdSd;
  __amdgpu_buffer_rsrc_t srdGs, srdGd; unsigned voffG, sofGs, voffGd; const LAS char* pG;
  __amdgpu_buffer_rsrc_t srdT; unsigned sofT2, m0W3, flag, voffF, voffZ; char* ws; int bid;
  __amdgpu_buffer_rsrc_t srdM, srdC; unsigned voffM, voffMs; bf16x8 mw[2]; f32x2 msc; float mv[8]; LAS f32x2* pM; const LAS float* pMr;
  unsigned voffS, sofSs, sofSd[4], m0S; const LAS char* pS; bf16x8 sf[2];
};
DI unsigned head_pos(unsigned nt, unsigned hd) { return hd == nt ? 0 : 1 + hd - (hd > nt); }
DI unsigned head_at(unsigned nt, unsigned g) { return g - 1 + (g - 1 >= nt); }
constexpr int w3_off(int p) { return ((p + 88) % 160) * 1024; }
DI const LAS char* ring_ptr(unsigned lanebase, int tile) { unsigned a = lanebase + (tile % NS2) * S2; PIN(a); return (const LAS char*)(size_t)a; }
template <int I, int HALF> DI void k2_dma1(const K2C& c) {
  constexpr int o = (I % NS2) * S2 + HALF * 1024;
  if constexpr (I < 10) dma16i<o, I * 64 * 8192 + HALF * 128>(c.m0T, c.voffW, c.srdW, c.sofW);
  else if constexpr (((I - 10) & 1) == 0) dma16i<o, ((I - 10) >> 1) * 16384 + HALF * 128, 1>(c.m0K, c.voffK, c.srdK, c.sofKV);
  else dma16i<o, ((I - 10) >> 1) * 16384 + HALF * 128, 1>(c.m0T, c.voffV, c.srdV, c.sofKV);
}
template <int I> DI void k2_dma(const K2C& c) { k2_dma1<I, 0>(c); k2_dma1<I, 1>(c); }
template <int I, int S_> DI void k2_ldh1(const K2C& c, bf16x8& r) { ld16c<S_ * 1024>(r, c.voffH, c.srdH, c.sofH + I * 4096); }
template <int H> DI void rdT(bf16x8& f, const LAS char* b, int ks, int dt) {
  const s16x4 v = __builtin_amdgcn_ds_read_tr16_b64_v4i16((LAS s16x4*)(b + ks * 8208 + (dt >> 1) * 512 + (dt & 1) * 8 + H * 256));
  u32x4 t = H ? __builtin_bit_cast(u32x4, f) : u32x4{}; const u32x2 x = __builtin_bit_cast(u32x2, v);
  if constexpr (H == 0) t = __builtin_shufflevector(x, x, 0, 1, -1, -1); else { t[2] = x[0]; t[3] = x[1]; }
  f = __builtin_bit_cast(bf16x8, t);
}
struct Quad { int kind, j, i; };
constexpr Quad quad_of(int Q) {
  if (Q < 40) return {0, Q >> 2, Q & 3};
  if (Q < 42) return {1, 0, Q - 40};
  const int T = (Q - 42) >> 3, r = (Q - 42) & 7;
  if (T < 7) return (r >> 1) == 0 ? Quad{1, 2 * T + 1, r & 1} : (r >> 1) == 1 ? Quad{2, 2 * T, r & 1} : (r >> 1) == 2 ? Quad{1, 2 * T + 2, r & 1} : Quad{2, 2 * T + 1, r & 1};
  return r < 2 ? Quad{1, 15, r} : r < 4 ? Quad{2, 14, r - 2} : r < 6 ? Quad{2, 15, r - 4} : Quad{3, 0, 0};
}
template <int Q, int M> DI void frag_ld(K2C& c) {
  constexpr Quad q = quad_of(Q); constexpr int E = M >> 1;
  if constexpr (M == 0 && q.i == 0) {
    if constexpr (q.kind == 0) c.pT = ring_ptr(c.aT, q.j);
    if constexpr (q.kind == 1 && (q.j & 1) == 0) c.pK = ring_ptr(c.aK, 10 + q.j);
    if constexpr (q.kind == 2 && (q.j & 1) == 0) c.pT = ring_ptr(c.aT, 11 + q.j);
  }
  if constexpr (q.kind == 0) rdT<(M & 1)>(c.f[Q % 3][E], c.pT, E & 1, 2 * q.i + (E >> 1));
  if constexpr (q.kind == 1 && (M & 1) == 0) c.f[Q % 3][E] = lds_rd(c.pK + (q.j & 1) * 8192 + q.i * 256 + E * 512);
  if constexpr (q.kind == 2) rdT<(M & 1)>(c.f[Q % 3][E], c.pT, q.j & 1, 4 * q.i + E);
}
template <int Q, int... Ms> DI void frag_ld_all(K2C& c, std::integer_sequence<int, Ms...>) { (frag_ld<Q, Ms>(c), ...); }
DI int imx3(int a, float b, float c) { return max(max(a, __float_as_int(b)), __float_as_int(c)); }
template <int U, int PART> DI void sm_max(K2C& c, f32x4 (&s)[2][2]) {
  if constexpr (PART == 0) c.mx[U] = max(imx3(__float_as_int(s[0][U][0]), s[0][U][1], s[0][U][2]), __float_as_int(s[0][U][3]));
  else c.mx[U] = imx3(imx3(c.mx[U], s[1][U][0], s[1][U][1]), s[1][U][2], s[1][U][3]);
  PIN(c.mx[U]);
}
template <int E> DI void sm_exp(f32x4 (&s)[2][2]) { float x = s[E >> 3][(E >> 2) & 1][E & 3]; asm volatile("v_exp_f32 %0, %0" : "+v"(x)); s[E >> 3][(E >> 2) & 1][E & 3] = x; }
template <int E> DI void sm_cvt(f32x4 (&s)[2][2], u32x4 (&p)[2]) {
  constexpr int kt = E >> 2, u = (E >> 1) & 1; unsigned r;
  asm volatile("v_cvt_pk_bf16_f32 %0, %1, %2" : "=v"(r) : "v"(s[kt][u][2 * (E & 1)]), "v"(s[kt][u][2 * (E & 1) + 1])); p[u][2 * kt + (E & 1)] = r;
}
template <int V> DI void sm_first(K2C& c, f32x4 (&s)[2][2]) {
  if constexpr (V == 1 || V == 2) sm_max<1, V - 1>(c, s);
  if constexpr (V == 3) {
    float t0, t1;
#define RMOVE(MX, F, S0, S1, NG) \
                 "v_mov_b32 %[t0], %[" MX "]\n\tv_mov_b32 %[t1], %[" MX "]\n\ts_nop 1\n\tv_permlane16_swap_b32 %[t0], %[t1]\n\ts_nop 1\n\tv_max_i32 %[t0], %[t0], %[t1]\n\t" \
                 "v_mov_b32 %[t1], %[t0]\n\ts_nop 1\n\tv_permlane32_swap_b32 %[t0], %[t1]\n\ts_nop 1\n\tv_max3_i32 %[t0], %[t0], %[t1], 0\n\t" \
                 "v_exp_f32 %[" F "], -%[t0]\n\tv_sub_f32 %[t1], 0, %[t0]\n\ts_nop 1\n\t" \
                 "v_mfma_f32_16x16x4_f32 %[" S0 "], %[one], %[t1], %[" S0 "]\n\tv_mfma_f32_16x16x4_f32 %[" S1 "], %[one], %[t1], %[" S1 "]\n\tv_mfma_f32_16x16x4_f32 %[" NG "], %[one], %[t1], %[" NG "]\n\t"
    asm volatile("v_max_i32 %[t0], %[m0], %[m1]\n\ts_mov_b32 %[pend], 0\n\tv_cmp_lt_i32 vcc, %[thr], %[t0]\n\ts_cbranch_vccz 1f\n\t"
                 RMOVE("m0", "f0", "s00", "s10", "n0") RMOVE("m1", "f1", "s01", "s11", "n1")
                 "s_mov_b32 %[pend], 1\n\ts_nop 15\n1:"
                 : [s00] "+v"(s[0][0]), [s01] "+v"(s[0][1]), [s10] "+v"(s[1][0]), [s11] "+v"(s[1][1]), [n0] "+v"(c.negm[0]), [n1] "+v"(c.negm[1]),
                   [f0] "+v"(c.fp[0]), [f1] "+v"(c.fp[1]), [pend] "=&s"(c.pend), [t0] "=&v"(t0), [t1] "=&v"(t1)
                 : [m0] "v"(c.mx[0]), [m1] "v"(c.mx[1]), [thr] "s"(__float_as_int(RTHR)), [one] "v"(c.aone) : "vcc", "memory");
#undef RMOVE
  }
  if constexpr (V >= 4) sm_exp<V - 4>(s);
}
template <int V> DI void sm_second(K2C& c, f32x4 (&s)[2][2], u32x4 (&p)[2], f32x4 (&n)[2][2], bool next) {
  if constexpr (V < 4) sm_exp<12 + V>(s);
  if constexpr (V >= 4 && V < 12) sm_cvt<V - 4>(s, p);
  if constexpr (V >= 14) { if (next) sm_max<0, V - 14>(c, n); }
}
DI void apply_pend(K2C& c) {
  asm volatile("" ::: "memory");
  if (__builtin_expect(c.pend != 0, 0)) {
    asm volatile("s_nop 7" : "+v"(c.o[6][0]), "+v"(c.o[6][1]), "+v"(c.o[7][0]), "+v"(c.o[7][1]), "+v"(c.o[5][0]), "+v"(c.o[5][1]), "+v"(c.o[4][0]), "+v"(c.o[4][1]));
#pragma unroll
    for (int dt = 0; dt < 8; ++dt) { c.o[dt][0] *= c.fp[0]; c.o[dt][1] *= c.fp[1]; }
    c.ls[0] *= c.fp[0]; c.ls[1] *= c.fp[1];
  }
}
enum { OP_NONE, OP_H, OP_D, OP_SL, OP_SS, OP_GL, OP_GS, OP_W3, OP_ML, OP_MS, OP_FL, OP_Z };
struct MemOp { int kind, a, b; };
constexpr int PER(int t, int x) { return (42 + 8 * t) * 8 + x; }
constexpr MemOp mem_op(int z) {
  if (z < 320) {
    const int I = z >> 5, X = z & 31;
    if (I == 0) { if ((X & 3) == 1) return {OP_H, 2 + (X >> 4), (X >> 2) & 3}; if (X == 27 || X == 31) return {OP_D, 3, X == 31}; return {OP_NONE, 0, 0}; }
    if (X < 16 && (X & 3) == 1) { if (I < 7) return {OP_H, I + 3, X >> 2}; if (I == 7) return {OP_D, 10 + (X >> 3), (X >> 2) & 1}; if (I == 8 && X < 8) return {OP_D, 13, X >> 2}; }
    if (X == 19 || X == 27) return {OP_D, I < 7 ? I + 3 : I == 7 ? 12 : I + 6, X == 27};
    if (I == 9 && X < 16 && (X & 3) == 1) return {OP_SL, X >> 2, 0};
    return {OP_NONE, 0, 0};
  }
  if (z < PER(0, 0)) return {OP_NONE, 0, 0};
  const int T = (z - PER(0, 0)) >> 6, X = (z - PER(0, 0)) & 63;
  if (X == 9 || X == 12) { if (T == 0) return {OP_D, 16, X == 12}; if (T <= 5) return {OP_D, 15 + 2 * T, X == 12}; }
  if ((X == 13 || X == 16) && T >= 1 && T <= 4) return {OP_D, 16 + 2 * T, X == 16};
  if (X == 18 || X == 34 || X == 50) {
    constexpr MemOp st[18] = {{OP_NONE, 0, 0}, {OP_NONE, 0, 0}, {OP_SS, 0, 0}, {OP_SS, 1, 0}, {OP_Z, 0, 0}, {OP_SS, 2, 0}, {OP_SS, 3, 0}, {OP_Z, 1, 0}, {OP_Z, 2, 0},
                              {OP_Z, 3, 0}, {OP_Z, 4, 0}, {OP_Z, 5, 0}, {OP_GS, 0, 0}, {OP_GS, 1, 0}, {OP_GS, 2, 0}, {OP_GS, 3, 0}, {OP_MS, 0, 0}, {OP_NONE, 0, 0}};
    if (T < 6) { const MemOp o = st[3 * T + (X >> 4) - 1]; return (o.kind == OP_Z && o.a >= 2) ? MemOp{OP_NONE, 0, 0} : o; }
  }
  if (T == 2 && (X == 41 || X == 42 || X == 44 || X == 45)) return {OP_GL, X == 41 ? 0 : X == 42 ? 1 : X - 42, 0};
  if (T == 1 && (X == 41 || X == 44)) return {OP_ML, X == 41 ? 1 : 2, 0};
  if (z == PER(5, 27)) return {OP_FL, 0, 0};
  if (T == 6 && (X & 7) == 5) return {OP_W3, X >> 3, 0};
  if (T == 7 && (X % 6) == 3 && X < 42) return {OP_W3, 8 + X / 6, 0};
  return {OP_NONE, 0, 0};
}
constexpr int younger(int z, int kind, int a) { int n = 0; for (int y = z - 1; y >= 0; --y) { const MemOp o = mem_op(y); if (o.kind == kind && o.a == a) return n; n += o.kind != OP_NONE; } return n; }
constexpr int imin(int a, int b) { return a < b ? a : b; }
constexpr int bq_wait(int i) { return imin(younger(32 * i + 16, OP_H, i + 1), younger(32 * i + 16, OP_D, i + 1)); }
constexpr int ba_wait(int t) { return imin(imin(younger(PER(t, 0), OP_D, 11 + 2 * t), t < 7 ? younger(PER(t, 0), OP_D, 12 + 2 * t) : younger(PER(t, 0), OP_W3, 3)), t == 4 ? younger(PER(t, 0), OP_SS, 3) : t == 6 ? younger(PER(t, 0), OP_FL, 0) : t == 7 ? younger(PER(t, 0), OP_MS, 0) : 63); }
static_assert(bq_wait(0) == 4 && bq_wait(3) == 10 && bq_wait(9) == 12 && ba_wait(0) == 10, "schedule");
template <int P> DI void side_ld(const K2C& c) { dma16i<P * 1024, P * 8 * D * 2, 1>(c.m0S, c.voffS, c.srdSs, c.sofSs); }
template <int P> DI void side_rd(K2C& c) { const LAS char* p = c.pS + (P >> 1) * 512 + (P & 1) * 32; c.sf[P & 1] = lds_tr(p, p + 256); }
template <int P> DI void side_st(const K2C& c) { st16<16>(__builtin_bit_cast(u32x4, c.sf[P & 1]), c.srdSd, c.voffH + c.sofSd[P]); }
template <int P> DI void side2_ld(const K2C& c) { dma16i<P * 1024, P * 8 * F * 2, 1>(c.m0S, c.voffG, c.srdGs, c.sofGs); }
template <int P> DI void side2_rd(K2C& c) { const LAS char* p = c.pG + (P >> 1) * 512 + (P & 1) * 8; c.sf[P & 1] = lds_tr(p, p + 256); }
template <int P> DI void side2_st(const K2C& c) { st16<16>(__builtin_bit_cast(u32x4, c.sf[P & 1]), c.srdGd, c.voffGd + (P >> 1) * 8192 + (P & 1) * 2048); }
template <int Q, int M> DI void k2_mem(K2C& c, bf16x8 (&hf)[10][4]) {
  constexpr int z = Q * 8 + M; constexpr MemOp o = mem_op(z);
  if constexpr (o.kind == OP_H) k2_ldh1<o.a, o.b>(c, hf[o.a][o.b]);
  if constexpr (o.kind == OP_D) k2_dma1<o.a, o.b>(c);
  if constexpr (o.kind == OP_Z) st16<16>((u32x4){~0u, ~0u, ~0u, ~0u}, mk_srd(c.ws + WS_PART, Z_END), c.voffZ + o.a * 1024);
  if constexpr (o.kind == OP_SL) side_ld<o.a>(c);
  if constexpr (o.kind == OP_SS) side_st<o.a>(c);
  if constexpr (o.kind == OP_GL) side2_ld<o.a>(c);
  if constexpr (o.kind == OP_GS) side2_st<o.a>(c);
  if constexpr (o.kind == OP_W3) dma16i<w3_off(8 * o.a), o.a * 8192, 2>(c.m0W3, c.voffH, c.srdT, c.sofT2);
  if constexpr (o.kind == OP_FL) asm volatile("buffer_load_dword %0, %1, %2, 0 offen offset:%3 sc1" : "=v"(c.flag) : "v"(c.voffF), "s"(c.srdC), "i"(o.a * 1024) : "memory");
  if constexpr (mem_op(z + 10).kind == OP_SS) { if constexpr (mem_op(z + 10).a == 0) asm volatile("s_waitcnt vmcnt(%0)" :: "i"(younger(z + 1, OP_SL, 3)) : "memory"); side_rd<mem_op(z + 10).a>(c); }
  if constexpr (mem_op(z + 10).kind == OP_GS) { if constexpr (mem_op(z + 10).a == 0) asm volatile("s_waitcnt vmcnt(%0)" :: "i"(younger(z + 1, OP_GL, 3)) : "memory"); side2_rd<mem_op(z + 10).a>(c); }
  if constexpr (o.kind == OP_ML) {
    asm volatile("buffer_load_dwordx4 %0, %1, %2, %3 offen nt" : "=v"(c.mw[o.a - 1]) : "v"(c.voffM), "s"(c.srdM), "s"((o.a - 1) * 6 * D * 2) : "memory");
  }
  if constexpr (z == PER(3, 6)) asm volatile("s_waitcnt vmcnt(%3)" : "+v"(c.msc), "+v"(c.mw[0]), "+v"(c.mw[1]) : "i"(younger(z + 1, OP_ML, 2)) : "memory");
  if constexpr (z >= PER(3, 6) && z < PER(3, 14)) {
    constexpr int e = z - PER(3, 6); const unsigned a = __builtin_bit_cast(u32x4, c.mw[0])[e >> 1], b = __builtin_bit_cast(u32x4, c.mw[1])[e >> 1];
    c.mv[e] = (e & 1) ? c.msc[0] * bfhi(a) + c.msc[1] * bfhi(b) : c.msc[0] * bflo(a) + c.msc[1] * bflo(b); PIN(c.mv[e]);
  }
  if constexpr (z >= PER(3, 38) && z < PER(3, 42)) { constexpr int e = z - PER(3, 38);
    auto r = __builtin_amdgcn_permlane32_swap(__float_as_uint(c.mv[e]), __float_as_uint(c.mv[4 + e]), false, false); c.mv[e] = __uint_as_float(r[0]) + __uint_as_float(r[1]); PIN(c.mv[e]); }
  if constexpr (z >= PER(3, 42) && z < PER(3, 44)) { constexpr int e = z - PER(3, 42);
    auto r = __builtin_amdgcn_permlane16_swap(__float_as_uint(c.mv[e]), __float_as_uint(c.mv[2 + e]), false, false); c.mv[e] = __uint_as_float(r[0]) + __uint_as_float(r[1]); PIN(c.mv[e]); }
  if constexpr (z == PER(3, 45)) *c.pM = (f32x2){c.mv[0], c.mv[1]};
  if constexpr (z == PER(5, 24)) { c.mv[0] = c.pMr[0]; c.mv[1] = c.pMr[512]; }
  if constexpr (o.kind == OP_MS) __builtin_amdgcn_raw_buffer_store_b32(__float_as_uint(sum816(c.mv[0] + c.mv[1])), c.srdC, c.voffMs, 0, 16);
}
template <int SI, int M> DI void q_pack(K2C& c) {
  constexpr int t = M >> 2, u = (M >> 1) & 1, e = M & 1; unsigned r;
  asm volatile("v_cvt_pk_bf16_f32 %0, %1, %2" : "=v"(r) : "v"(c.o[2 * SI + t][u][2 * e]), "v"(c.o[2 * SI + t][u][2 * e + 1]));
  u32x4 v = __builtin_bit_cast(u32x4, c.qf[SI][u]); v[2 * t + e] = r; c.qf[SI][u] = __builtin_bit_cast(bf16x8, v);
}
template <int... Ms> DI void q_pack_last(K2C& c, std::integer_sequence<int, Ms...>) { (q_pack<3, Ms>(c), ...); }
template <int Q, int M> DI void k2_slot(K2C& c, bf16x8 (&hf)[10][4]) {
  constexpr Quad q = quad_of(Q);
  bf16x8 (&f)[4] = c.f[Q % 3];
  if constexpr (q.kind == 0) { constexpr int dt = 2 * q.i + (M >> 2), u = (M >> 1) & 1, ks = M & 1; MFMA16_ACC(c.o[dt][u], f[2 * (M >> 2) + ks], hf[q.j][2 * ks + u]); }
  if constexpr (q.kind == 1) { constexpr int u = M >> 2, e = M & 3; f32x4& s = (q.j & 1) ? c.sB[q.i][u] : c.sA[q.i][u];
    if constexpr (e == 0) asm volatile("v_mfma_f32_16x16x32_bf16 %0, %1, %2, %3" : "=&v"(s) : "v"(f[0]), "v"(c.qf[0][u]), "v"(c.negm[u])); else MFMA16_ACC(s, f[e], c.qf[e][u]); }
  if constexpr (q.kind == 2) { constexpr int e = M >> 1, u = M & 1;
    if constexpr (q.j == 0) asm volatile("v_mfma_f32_16x16x32_bf16 %0, %1, %2, 0" : "=&v"(c.o[4 * q.i + e][u]) : "v"(f[e]), "v"(c.pA[u]));
    else MFMA16_ACC(c.o[4 * q.i + e][u], f[e], (q.j & 1) ? c.pB[u] : c.pA[u]); }
  SBAR();
  frag_ld<Q + 2, M>(c);
  k2_mem<Q, M>(c, hf);
  if constexpr (q.kind == 0 && q.i == 2 && q.j < 9) {
    const u32x4 t = __builtin_bit_cast(u32x4, hf[q.j + 1][M >> 1]);
    if constexpr (M == 0) asm volatile("v_max_u32 %0, %1, %2" : "=v"(c.chk) : "v"(t[0]), "v"(t[1]));
    else asm volatile("v_max3_u32 %0, %0, %1, %2" : "+v"(c.chk) : "v"(t[2 * (M & 1)]), "v"(t[2 * (M & 1) + 1]));
  }
  if constexpr (Q >= 37 && Q < 40) q_pack<Q - 37, M>(c);
  if constexpr (Q >= 42) {
    if constexpr (q.kind == 1) { if constexpr (q.j & 1) sm_second<8 * q.i + M>(c, c.sA, c.pA, c.sB, true); else sm_second<8 * q.i + M>(c, c.sB, c.pB, c.sA, true); }
    if constexpr (q.kind == 2 && q.j < 15) { if constexpr (q.j & 1) sm_first<8 * q.i + M>(c, c.sA); else sm_first<8 * q.i + M>(c, c.sB); }
  }
  SBAR();
}
template <int... Vs> DI void sm_second_all(K2C& c, std::integer_sequence<int, Vs...>) { (sm_second<Vs>(c, c.sB, c.pB, c.sA, false), ...); }
template <int Q> DI void k2_quad(K2C& c, bf16x8 (&hf)[10][4]) {
  constexpr Quad q = quad_of(Q);
  if constexpr (q.kind == 0 && q.i == 2) {
    if constexpr (q.j < 9) wait_bar4v<bq_wait(q.j)>(hf[q.j + 1][0], hf[q.j + 1][1], hf[q.j + 1][2], hf[q.j + 1][3]);
    else wait_barv<bq_wait(9)>();
  }
  if constexpr (q.kind == 0 && q.i == 3 && q.j < 9) {
    asm volatile("" ::: "memory");
    reload4<1024>(c.chk, hf[q.j + 1][0], hf[q.j + 1][1], hf[q.j + 1][2], hf[q.j + 1][3], c.voffH, c.srdH, c.sofH + (q.j + 1) * 4096);
  }
  if constexpr (q.kind == 1 && (q.j & 1) && q.i == 0) {
    wait_barv<ba_wait(q.j >> 1)>();
    if constexpr (q.j == 9) { if (threadIdx.x == 0) cnt_add(c.ws, CNT_SA + (c.bid & 7)); asm volatile("" ::: "memory"); }
    if constexpr (q.j == 13) { PIN(c.flag); shards_wait(c.flag, c.ws, CNT_SA, c.voffH >> 4); }
    if constexpr (q.j == 15) { if (threadIdx.x == 0) cnt_add(c.ws, CNT_SB + (c.bid & 7)); asm volatile("" ::: "memory"); }
  }
  if constexpr (q.kind == 2 && q.j == 15 && q.i == 0) { sm_second_all(c, std::make_integer_sequence<int, 16>{}); asm volatile("s_nop 1"); }
  if constexpr (Q < 40) __builtin_amdgcn_s_setprio(3 - ((Q + 2) & 3));
  else if constexpr (Q >= 42) { constexpr int r = (Q - 42) & 7; if constexpr (r == 0) __builtin_amdgcn_s_setprio(3); if constexpr (r == 3) __builtin_amdgcn_s_setprio(2);
                                if constexpr (r == 6) __builtin_amdgcn_s_setprio(1); if constexpr (r == 7) __builtin_amdgcn_s_setprio(0); }
  asm volatile("" :: "v"(c.f[Q % 3][0]), "v"(c.f[Q % 3][1]), "v"(c.f[Q % 3][2]), "v"(c.f[Q % 3][3]));
  SBAR();
  k2_slot<Q, 0>(c, hf); k2_slot<Q, 1>(c, hf); k2_slot<Q, 2>(c, hf); k2_slot<Q, 3>(c, hf);
  if constexpr (q.kind == 2) {
    if constexpr (q.j == 15) { if constexpr (q.i == 0) { MFMA16_ACC(c.ls[0], c.ones, c.pB[0]); MFMA16_ACC(c.ls[1], c.ones, c.pB[1]); } }
    else if constexpr (q.j == 0) asm volatile("v_mfma_f32_16x16x32_bf16 %0, %1, %2, 0" : "=&v"(c.ls[q.i]) : "v"(c.ones), "v"(c.pA[q.i]));
    else MFMA16_ACC(c.ls[q.i], c.ones, (q.j & 1) ? c.pB[q.i] : c.pA[q.i]);
    SBAR();
  }
  k2_slot<Q, 4>(c, hf); k2_slot<Q, 5>(c, hf); k2_slot<Q, 6>(c, hf); k2_slot<Q, 7>(c, hf);
  if constexpr (q.kind == 2 && q.i == 1 && q.j < 15) apply_pend(c);
}
template <int Q0, int... Qs> DI void k2_quads(K2C& c, bf16x8 (&hf)[10][4], std::integer_sequence<int, Qs...>) { (k2_quad<Q0 + Qs>(c, hf), ...); }

DI void k2_attention(const u16* __restrict__ x, const u16* __restrict__ cnd, const u16* __restrict__ wq, const u16* __restrict__ kc, const u16* __restrict__ vc, const u16* __restrict__ wo, const u16* __restrict__ wd,
                     const u16* __restrict__ wg, const u16* __restrict__ wu, const u16* __restrict__ wmod, char* __restrict__ ws, char* smem, unsigned long long& tag, bf16x8 (&ofr)[8]) {
  const int tid = threadIdx.x, lane = tid & 63, bid = blockIdx.x;
  const unsigned w = rfl(tid >> 6);
  const int head = ((bid & 7) >> 1) * 8 + ((bid >> 3) & 7), qb = (bid & 1) * 4 + (bid >> 6);
  const unsigned gsl = bid / 5, gkp = bid - gsl * 5, gk = gkp * 128 + w * 16 + 2 * (lane >> 3);
  bf16x8 xv[2], gw[2]; unsigned gc;
  asm volatile("s_load_dwordx2 %0, %1, %2" : "=s"(tag) : "s"(ws), "i"((int)WS_EP));
  {
    const __amdgpu_buffer_rsrc_t srdG = mk_srd(wmod, bid < 100 ? D * 6 * D * 2 : 0), srdN = mk_srd(cnd, D * 2);
    asm volatile("s_nop 4\n\tbuffer_load_dwordx4 %0, %3, %4, 0 offen nt\n\tbuffer_load_dwordx4 %1, %3, %4, %5 offen nt\n\tbuffer_load_dword %2, %6, %7, 0 offen"
                 : "=&v"(gw[0]), "=&v"(gw[1]), "=&v"(gc) : "v"((gk * 6 * D + gsl * 64 + (lane & 7) * 8) * 2), "s"(srdG), "s"(6 * D * 2), "v"(gk * 2), "s"(srdN) : "memory");
  }
  SBAR();
  const int hrow = qb * 256 + head * 8 + (lane & 7), hch = lane >> 3;
  const unsigned ho0 = h_off(hrow, w * 10 + hch), ho1 = lane < 16 ? h_off(hrow, w * 10 + 8 + hch) : 0x7fffffffu;
  K2C c;
  const unsigned sb = (unsigned)(size_t)(LAS char*)smem;
  c.srdH = mk_srd(ws + WS_H, S * D * 2); c.srdC = mk_srd(ws, WS_H);
  st16<16>((u32x4){~0u, ~0u, ~0u, ~0u}, c.srdH, ho0); st16<16>((u32x4){~0u, ~0u, ~0u, ~0u}, c.srdH, ho1);
  const unsigned gdst = (bid < 100 && lane < 8) ? WS_MOD + (gkp * 2 * D + gsl * 64 + w * 8 + lane) * 4 : 0x7fffffffu;
  __builtin_amdgcn_raw_buffer_store_b32(lane < 8 ? ~0u : 0u, c.srdC, (tid >= 8 && tid < 13) ? WS_CNT + (bid * 5 + tid - 8) * 128 : gdst, 0, 16);
  {
    const __amdgpu_buffer_rsrc_t srdX = mk_srd(x + (size_t)(qb * 256 + head * 8 + w) * D, D * 2); ld16<0>(xv[0], lane * 16, srdX, 0); ld16<1024>(xv[1], lane * 16, srdX, 0); }
  {
    const unsigned r = (((bid - 96) >> 3) * 2 + (bid & 1)) * 16 + (tid >> 5);
    asm volatile("s_mov_b32 m0, %0\n\ts_nop 0\n\tbuffer_load_dword %1, %2, 0 offen lds" :: "s"(sb + L2_SIDE + 45056), "v"(r * 8192 + ((bid & 7) >> 1) * 2048 + (tid & 31) * 64), "s"(mk_srd(wq, bid >= 96 ? D * DQ * 2 : 0)) : "memory");
  }
  c.pM = (LAS f32x2*)(size_t)(sb + L2_SIDE + 32768 + (w * 2 + ((lane >> 3) & 1)) * 256 + ((lane & 7) * 8 + (lane >> 5) * 4 + ((lane >> 4) & 1) * 2) * 4);
  c.pMr = (const LAS float*)(size_t)(sb + L2_SIDE + 32768 + (lane >> 3) * 256 + (w * 8 + (lane & 7)) * 4);
  SBAR();
  asm volatile("s_waitcnt vmcnt(%4) lgkmcnt(0)" : "+v"(gw[0]), "+v"(gw[1]), "+v"(gc), "+s"(tag) : "i"(6) : "memory");
  tag += 1;
  { const float a = bflo(gc), b = bfhi(gc); c.msc = (f32x2){rbf(a * __builtin_amdgcn_rcpf(1.f + __expf(-a))), rbf(b * __builtin_amdgcn_rcpf(1.f + __expf(-b)))}; }
  if (bid < 100) {
    const u32x4 a = __builtin_bit_cast(u32x4, gw[0]), b = __builtin_bit_cast(u32x4, gw[1]);
    float v[8];
#pragma unroll
    for (int e = 0; e < 4; ++e) { v[2 * e] = c.msc[0] * bflo(a[e]) + c.msc[1] * bflo(b[e]); v[2 * e + 1] = c.msc[0] * bfhi(a[e]) + c.msc[1] * bfhi(b[e]); }
#pragma unroll
    for (int e = 0; e < 4; ++e) { auto r = __builtin_amdgcn_permlane32_swap(__float_as_uint(v[e]), __float_as_uint(v[4 + e]), false, false); v[e] = __uint_as_float(r[0]) + __uint_as_float(r[1]); }
#pragma unroll
    for (int e = 0; e < 2; ++e) { auto r = __builtin_amdgcn_permlane16_swap(__float_as_uint(v[e]), __float_as_uint(v[2 + e]), false, false); v[e] = __uint_as_float(r[0]) + __uint_as_float(r[1]); }
    *c.pM = (f32x2){v[0], v[1]};
  }
  asm volatile("s_waitcnt vmcnt(%0) lgkmcnt(0)\n\ts_barrier" :: "i"(3) : "memory");
  const u32x2 tg = {(unsigned)tag, (unsigned)(tag >> 32)};
  __builtin_amdgcn_raw_buffer_store_b64(tg, c.srdC, tid < 32 ? WS_TAG + lane * 2048 + bid * 8 : 0x7fffffffu, 0, 16);
  __builtin_amdgcn_raw_buffer_store_b32(min(__float_as_uint(sum816(c.pMr[0] + c.pMr[512])), 0xfffffffeu), c.srdC, gdst, 0, 16);
  asm volatile("s_waitcnt vmcnt(%2)" : "+v"(xv[0]), "+v"(xv[1]) : "i"(3) : "memory");
  LAS char* xim = (LAS char*)(size_t)(sb + L2_SIDE + 1024);
  {
    const u32x4 p = __builtin_bit_cast(u32x4, xv[0]), q = __builtin_bit_cast(u32x4, xv[1]);
    *(LAS u32x4*)(xim + w * 4096 + w * 16 + lane * 16) = p; *(LAS u32x4*)(xim + w * 4096 + w * 16 + 1024 + lane * 16) = q;
    float s0 = 0.f, q0 = 0.f, q1 = 0.f;
#pragma unroll
    for (int e = 0; e < 4; ++e) s0 += (bflo(p[e]) + bfhi(p[e])) + (bflo(q[e]) + bfhi(q[e]));
    const float mu = sum64(s0) * (1.f / D);
#pragma unroll
    for (int e = 0; e < 4; ++e) { q0 += (bflo(p[e]) - mu) * (bflo(p[e]) - mu) + (bfhi(p[e]) - mu) * (bfhi(p[e]) - mu); q1 += (bflo(q[e]) - mu) * (bflo(q[e]) - mu) + (bfhi(q[e]) - mu) * (bfhi(q[e]) - mu); }
    *(LAS f32x2*)(xim + w * 4096 + 2560) = (f32x2){mu, rsqrtf(sum64(lane < 16 ? q0 + q1 : q0) * (1.f / D) + EPS) * QSCALE};
  }
  SBAR();
  const unsigned row = (lane >> 2) & 7, sub = lane >> 5, ch4 = lane & 3;
  c.srdW = mk_srd(wq, D * DQ * 2); c.voffW = row * 8192 + sub * 64 + ch4 * 16; c.m0T = sb + (w >> 1) * 4104 + (w & 1) * 2056; c.sofW = w * 8 * 8192 + head * 256;
  if (bid >= 100) { k2_dma<0>(c); k2_dma<1>(c); }
  SBAR();
  c.m0S = sb + L2_SIDE + w * 4096;
  bf16x8 hf[10][4];
  {
    asm volatile("s_waitcnt lgkmcnt(0)\n\ts_barrier" ::: "memory");
    f32x2 xn[2][4];
    {
      const LAS char* xr = xim + (lane & 7) * 4096;
      const f32x2 lnp = *(const LAS f32x2*)(xr + 2560);
#pragma unroll
      for (int it = 0; it < 2; ++it) {
        const u32x4 xx = *(const LAS u32x4*)(xr + (lane & 7) * 16 + (w * 10 + (it ? 8 + (hch & 1) : hch)) * 16);
#pragma unroll
        for (int e = 0; e < 4; ++e) { xn[it][e] = ((f32x2){bflo(xx[e]), bfhi(xx[e])} - lnp[0]) * lnp[1]; PIN(xn[it][e]); }
      }
    }
    if (w == 0) {
      for (unsigned spins = 0; spins < (1u << 18); ++spins) {
        const u32x4 a = __builtin_amdgcn_raw_buffer_load_b128(c.srdC, WS_TAG + (bid & 31) * 2048 + lane * 16, 0, 16), b = __builtin_amdgcn_raw_buffer_load_b128(c.srdC, WS_TAG + (bid & 31) * 2048 + 1024 + lane * 16, 0, 16);
        if (__all(a[0] == tg[0] && a[1] == tg[1] && a[2] == tg[0] && a[3] == tg[1] && b[0] == tg[0] && b[1] == tg[1] && b[2] == tg[0] && b[3] == tg[1])) break;
        asm volatile("s_sleep 1" ::: "memory");
      }
    }
    asm volatile("s_waitcnt lgkmcnt(0)\n\ts_barrier" ::: "memory");
    const int ml = min(lane, 39);
    f32x4 mp[5];
    {
      const unsigned vo = WS_MOD + (ml < 20 ? w * 80 + ml * 4 : D + w * 80 + (ml - 20) * 4) * 4; unsigned spins = 0;
      do asm volatile("buffer_load_dwordx4 %0, %5, %6, 0 offen sc1\n\tbuffer_load_dwordx4 %1, %5, %6, %7 offen sc1\n\tbuffer_load_dwordx4 %2, %5, %6, %8 offen sc1\n\t"
                      "buffer_load_dwordx4 %3, %5, %6, %9 offen sc1\n\tbuffer_load_dwordx4 %4, %5, %6, %10 offen sc1\n\ts_waitcnt vmcnt(0)"
                      : "=&v"(mp[0]), "=&v"(mp[1]), "=&v"(mp[2]), "=&v"(mp[3]), "=&v"(mp[4]) : "v"(vo), "s"(c.srdC), "s"(5120), "s"(10240), "s"(15360), "s"(20480) : "memory");
      while (__any(umax4(__builtin_bit_cast(u32x4, mp[0]), umax4(__builtin_bit_cast(u32x4, mp[1]), umax4(__builtin_bit_cast(u32x4, mp[2]), umax4(__builtin_bit_cast(u32x4, mp[3]), umax4(__builtin_bit_cast(u32x4, mp[4]), 0u))))) == 0xffffffffu)
             && ++spins < SPIN_CAP);
    }
    f32x4 mv = ((mp[0] + mp[1]) + (mp[2] + mp[3])) + mp[4];
#pragma unroll
    for (int e = 0; e < 4; ++e) { const float r = rbf(mv[e]); mv[e] = ml < 20 ? r * QSCALE : rbf(1.f + r); }
    LAS char* ab = (LAS char*)(size_t)c.m0S;
    *(LAS f32x4*)(ab + ml * 16) = mv;
#pragma unroll
    for (int it = 0; it < 2; ++it) {
      const int ch = it ? 8 + (hch & 1) : hch;
      const f32x4 b0 = *(const LAS f32x4*)(ab + ch * 32), b1 = *(const LAS f32x4*)(ab + ch * 32 + 16), a0 = *(const LAS f32x4*)(ab + 320 + ch * 32), a1 = *(const LAS f32x4*)(ab + 336 + ch * 32);
      const f32x2 h0 = xn[it][0] * (f32x2){a0[0], a0[1]} + (f32x2){b0[0], b0[1]}, h1 = xn[it][1] * (f32x2){a0[2], a0[3]} + (f32x2){b0[2], b0[3]};
      const f32x2 h2 = xn[it][2] * (f32x2){a1[0], a1[1]} + (f32x2){b1[0], b1[1]}, h3 = xn[it][3] * (f32x2){a1[2], a1[3]} + (f32x2){b1[2], b1[3]};
      const u32x4 o = {pk2(h0[0], h0[1]), pk2(h1[0], h1[1]), pk2(h2[0], h2[1]), pk2(h3[0], h3[1])};
      st16<16>(nopoison(o), c.srdH, it ? ho1 : ho0);
    }
  }
  if (bid < 100) { k2_dma<0>(c); k2_dma<1>(c); }
  k2_dma<2>(c);
  c.voffH = lane * 16; c.sofH = (qb * 8 + w) * 40 * 1024;
  asm volatile("s_barrier" ::: "memory");
  k2_ldh1<0, 0>(c, hf[0][0]); k2_ldh1<0, 1>(c, hf[0][1]); k2_ldh1<0, 2>(c, hf[0][2]); k2_ldh1<0, 3>(c, hf[0][3]);
  asm volatile("s_barrier" ::: "memory");
  k2_ldh1<1, 0>(c, hf[1][0]); k2_ldh1<1, 1>(c, hf[1][1]); k2_ldh1<1, 2>(c, hf[1][2]); k2_ldh1<1, 3>(c, hf[1][3]);
  SBAR();
  c.srdK = mk_srd(kc, NH * SK * DH * 2); c.srdV = mk_srd(vc, NH * SK * DH * 2);
  {
    c.voffV = row * 256 + sub * 64 + ch4 * 16;
    c.voffK = row * 256 + sub * 64 + ((ch4 ^ (w & 2)) * 16);
    c.m0K = sb + w * 2048;
    c.sofKV = (head * SK + (SK - W) + w * 8) * 256;
    const int g = lane >> 4, q = (lane >> 2) & 3, p = lane & 3;
    c.aT = sb + (g >> 1) * 4104 + (g & 1) * 2056 + q * 64 + p * 16;
    c.aK = sb + 2048 * q + 64 * p + 16 * (g ^ (q & 2));
    const unsigned u = bid * 8 + w; const bool isO = u < 1280, live = u < 1820;
    const unsigned uu = isO ? u : u - 1280, ks = uu / 10, nb = uu - ks * 10;
    c.srdSs = mk_srd(isO ? wo : wd, live ? (isO ? DQ * D * 2 : F * D * 2) : 0);
    c.srdSd = mk_srd(ws + (isO ? WS_WOT : WS_WDT), live ? (isO ? DQ * D * 2 : F * D * 2) : 0);
    c.voffS = row * (D * 2) + sub * 64 + ch4 * 16;
    c.sofSs = ks * 32 * (D * 2) + nb * 128;
#pragma unroll
    for (int i = 0; i < 4; ++i) { const unsigned C = 4 * nb + i, nt = C / 5, cc = C - nt * 5;
      c.sofSd[i] = (isO ? (((nt * 4 + (ks >> 5)) * 8 + head_pos(nt, (ks >> 2) & 7)) * 5 + cc) * 4 + (ks & 3) : (nt * 54 + ks) * 5 + cc) * 1024; }
    {
      const unsigned m = u >= 540, r = u - m * 540, k2 = r / 27, jg = r - k2 * 27; const bool on = u < 1080;
      c.srdGs = mk_srd(m ? wu : wg, on ? D * F * 2 : 0); c.srdGd = mk_srd(ws + WS_WGU, on ? D * F * 4 : 0);
      c.voffG = row * (F * 2) + sub * 64 + ch4 * 16; c.sofGs = k2 * 32 * (F * 2) + jg * 128;
      c.voffGd = ((jg * 10 + (k2 >> 1)) * 16 + m * 4 + (k2 & 1)) * 1024 + lane * 16;
      c.pG = (const LAS char*)(size_t)(sb + L2_SIDE + w * 4096 + g * 1024 + q * 64 + p * 16);
    }
    c.srdT = mk_srd(ws + WS_WOT, DQ * D * 2); c.sofT2 = ((head & 7) * 4 + (head >> 3)) * 163840 + w * 1024;
    c.m0W3 = sb + w * 1024; c.voffF = WS_CNT + (CNT_SA + (lane & 7)) * 128; c.ws = ws; c.bid = bid; c.voffZ = Z_Y + u * 2048 + lane * 16;
    {
      const unsigned sl = bid / 5, kp = bid - sl * 5, k = kp * 128 + w * 16 + 2 * (lane >> 3);
      c.srdM = mk_srd(wmod, bid < 200 ? D * 6 * D * 2 : 0);
      c.voffM = (k * 6 * D + 2 * D + sl * 64 + (lane & 7) * 8) * 2;
      c.voffMs = (bid < 200 && lane < 8) ? WS_MOD2 + (kp * 2560 + sl * 64 + w * 8 + lane) * 4 : 0x7fffffffu;
    }
    c.pS = (const LAS char*)(size_t)(c.m0S + g * 1024 + q * 64 + p * 8);
  }
  PIN(c.voffV); PIN(c.voffK); PIN(c.aT); PIN(c.aK); PIN(c.voffS); PIN(c.voffG); PIN(c.voffGd); PIN(c.voffF); PIN(c.voffM); PIN(c.voffMs);
  asm volatile("" : "+s"(c.sofSs), "+s"(c.sofSd[0]), "+s"(c.sofSd[1]), "+s"(c.sofSd[2]), "+s"(c.sofSd[3]), "+s"(c.sofGs), "+s"(c.sofT2));
#pragma unroll
  for (int dt = 0; dt < 16; ++dt) { c.o[dt >> 1][dt & 1] = f32x4{}; PIN(c.o[dt >> 1][dt & 1]); }
  asm volatile("s_waitcnt vmcnt(4)" : "+v"(hf[0][0]), "+v"(hf[0][1]), "+v"(hf[0][2]), "+v"(hf[0][3]) :: "memory");
  reload4<1024>(umax16(hf[0][0], hf[0][1], hf[0][2], hf[0][3]), hf[0][0], hf[0][1], hf[0][2], hf[0][3], c.voffH, c.srdH, c.sofH);
  asm volatile("s_barrier" ::: "memory");
  frag_ld_all<0>(c, std::make_integer_sequence<int, 8>{}); frag_ld_all<1>(c, std::make_integer_sequence<int, 8>{});
  SBAR();
  k2_quads<0>(c, hf, std::make_integer_sequence<int, 20>{});
  k2_quads<20>(c, hf, std::make_integer_sequence<int, 20>{});
  asm volatile("s_nop 7" ::: "memory"); SBAR();
  q_pack_last(c, std::make_integer_sequence<int, 8>{});
  c.negm[0] = c.negm[1] = f32x4{};
  SBAR();
  asm volatile("s_nop 1" : "+v"(c.negm[0]), "+v"(c.negm[1]), "+v"(c.qf[0][0]), "+v"(c.qf[3][1]));
  k2_quad<40>(c, hf); k2_quad<41>(c, hf);
  asm volatile("s_nop 7" ::: "memory"); SBAR();
  c.aone = lane < 16 ? 1.f : 0.f; c.ones = (u32x4){0x3f803f80u, 0x3f803f80u, 0x3f803f80u, 0x3f803f80u}; PIN(c.ones);
#pragma unroll
  for (int u = 0; u < 2; ++u) {
    const f32x4 a = c.sA[0][u], b = c.sA[1][u];
    const float m = max1632(fmaxf(fmaxf(fmaxf(a[0], a[1]), fmaxf(a[2], a[3])), fmaxf(fmaxf(b[0], b[1]), fmaxf(b[2], b[3]))));
    c.sA[0][u] -= m; c.sA[1][u] -= m; c.negm[u] = (f32x4){-m, -m, -m, -m}; PIN(c.negm[u]); c.fp[u] = 1.f;
#pragma unroll
    for (int r = 0; r < 4; ++r) { c.sA[0][u][r] = EX2(c.sA[0][u][r]); if (u == 0) c.sA[1][0][r] = EX2(c.sA[1][0][r]); }
  }
  SBAR();
  k2_quads<42>(c, hf, std::make_integer_sequence<int, 61>{});
  const __amdgpu_buffer_rsrc_t srdO = mk_srd(ws + WS_O, S * DQ * 2);
  const unsigned vo = ((qb * 8 + w) * 128 + head * 4) * 2048 + lane * 16;
  asm volatile("s_nop 3" :: "v"(c.f[103 % 3][0]), "v"(c.f[103 % 3][1]), "v"(c.f[103 % 3][2]), "v"(c.f[103 % 3][3]));
  float il[2] = {__builtin_amdgcn_rcpf(c.ls[0][0]), __builtin_amdgcn_rcpf(c.ls[1][0])};
  PIN(il[0]); PIN(il[1]);
  SBAR();
  auto o_out = [&](int i) { ofr[i] = pack44(c.o[i & ~1][i & 1], c.o[i | 1][i & 1], il[i & 1]); st16<16>(__builtin_bit_cast(u32x4, ofr[i]), srdO, vo + i * 1024); SBAR(); };
  k2_slot<103, 0>(c, hf); k2_slot<103, 1>(c, hf); o_out(0); k2_slot<103, 2>(c, hf); k2_slot<103, 3>(c, hf); o_out(1);
  k2_slot<103, 4>(c, hf); k2_slot<103, 5>(c, hf); o_out(2); k2_slot<103, 6>(c, hf); k2_slot<103, 7>(c, hf); o_out(3);
  __builtin_amdgcn_s_setprio(0);
  o_out(4); o_out(5); o_out(6); o_out(7);
}

constexpr int RLD = 84;
constexpr int L_OWN = 131072, L_CS = L_OWN, L_W5X = L_OWN + 1024, L_MOD = L_OWN + 30720, LDS3 = L_MOD + 2048;
struct K3C {
  __amdgpu_buffer_rsrc_t srdO, srdM, srdX; unsigned voff, sofO, voffM, voffX, voffX1, voffXr[2], voffF, flag; int nt; char* ws;
  __amdgpu_buffer_rsrc_t srdG; unsigned m04, sof4;
  __amdgpu_buffer_rsrc_t srdP; unsigned voffP; LAS char* pOwn; bool owner;
  const LAS char* pw[3]; bf16x8 f[3][4], xv[2]; f32x4 acc[2][5]; float mp[5];
};
template <int G, int E> DI void k3_lo(const K3C& c, bf16x8& r) { ld16c<(E & 3) * 1024>(r, c.voff, c.srdO, c.sofO + head_at(c.nt, G) * 8192 + (E >> 2) * 4096); }
template <int Q, int E> DI void k3_rd(K3C& c) { if constexpr (Q < 40) { constexpr int o = w3_off(4 * Q + E); c.f[Q % 3][E] = lds_rd(c.pw[o >> 16] + (o & 65535)); } }
enum { P3_NONE, P3_O, P3_W4, P3_MP, P3_X };
constexpr MemOp k3_op(int z) {
  const int G = z / 40, X = z % 40;
  if (G == 1 && (X & 1) && X < 32) return {P3_O, 2 + (X >> 4), (X >> 1) & 7};
  if (G >= 2 && G < 6 && (X & 3) == 1 && X < 32) return {P3_O, G + 2, X >> 2};
  if ((G == 4 || G == 5) && (X == 33 || X == 37)) return {P3_W4, 2 * (G - 4) + (X == 37), 0};
  if (G == 6 && X % 6 == 1 && X < 36) return {P3_W4, 4 + X / 6, 0};
  if (G == 6 && X % 6 == 4 && X < 30) return {P3_MP, X / 6, 0};
  if (G == 6 && (X == 34 || X == 37)) return {P3_X, X == 37, 0};
  return {P3_NONE, 0, 0};
}
constexpr int k3_younger(int z, int kind, int a) { int n = 0; for (int y = z - 1; y >= 40; --y) { const MemOp o = k3_op(y); if (o.kind == kind && o.a == a) return n; n += o.kind != P3_NONE; } return n; }
template <int CC> DI void k3_out(K3C& c) {
  const auto r0 = __builtin_amdgcn_permlane16_swap(pk2(c.acc[0][CC][0], c.acc[0][CC][1]), pk2(c.acc[1][CC][0], c.acc[1][CC][1]), false, false);
  const auto r1 = __builtin_amdgcn_permlane16_swap(pk2(c.acc[0][CC][2], c.acc[0][CC][3]), pk2(c.acc[1][CC][2], c.acc[1][CC][3]), false, false);
  st16<16>((u32x4){r0[0], r1[0], r0[1], r1[1]}, c.srdP, c.voffP + CC * 256);
  if (c.owner) { *(LAS f32x4*)(c.pOwn + CC * 64) = c.acc[0][CC]; *(LAS f32x4*)(c.pOwn + CC * 64 + 16 * RLD * 4) = c.acc[1][CC]; }
}
template <int Q, int M> DI void k3_slot(K3C& c, bf16x8 (&of)[8][8]) {
  constexpr int G = Q / 5, cc = Q % 5, u = M >> 2, e = M & 3, X = cc * 8 + M;
  if constexpr (G == 0 && e == 0) asm volatile("v_mfma_f32_16x16x32_bf16 %0, %1, %2, 0" : "=&v"(c.acc[u][cc]) : "v"(c.f[Q % 3][0]), "v"(of[0][u]));
  else MFMA16_ACC(c.acc[u][cc], c.f[Q % 3][e], of[G][2 * e + u]);
  SBAR();
  if constexpr ((M & 1) == 0) k3_rd<Q + 2, (M >> 1)>(c);
  constexpr MemOp o = k3_op(40 * G + X);
  if constexpr (o.kind == P3_O) k3_lo<o.a, o.b>(c, of[o.a][o.b]);
  if constexpr (o.kind == P3_W4) dma16i<(((o.a >> 1) + 6) & 7) * SLOT + (o.a & 1) * 1024, (o.a >> 1) * 16384 + (o.a & 1) * 1024, 2>(c.m04, c.voff, c.srdG, c.sof4);
  if constexpr (o.kind == P3_X) ld16<0>(c.xv[o.a], c.voffXr[o.a], c.srdX, 0);
  if constexpr (o.kind == P3_MP) asm volatile("buffer_load_dword %0, %1, %2, %3 offen sc1" : "=v"(c.mp[o.a]) : "v"(c.voffM), "s"(c.srdM), "s"((unsigned)WS_MOD2 + o.a * 10240) : "memory");
  SBAR();
}
template <int Q> DI void k3_quad(K3C& c, bf16x8 (&of)[8][8]) {
  constexpr int G = Q / 5;
  if constexpr (Q % 5 == 0 && G >= 1) {
    bf16x8 (&o)[8] = of[G];
    constexpr int n = k3_younger(40 * G, P3_O, G);
    asm volatile("s_waitcnt vmcnt(%8)" : "+v"(o[0]), "+v"(o[1]), "+v"(o[2]), "+v"(o[3]), "+v"(o[4]), "+v"(o[5]), "+v"(o[6]), "+v"(o[7]) : "i"(n) : "memory");
    if constexpr (G == 1) { PIN(c.flag); shards_wait(c.flag, c.ws, CNT_SB, c.voff >> 4); }
    if constexpr (G == 4 || G == 6) asm volatile("s_barrier" ::: "memory");
    __builtin_amdgcn_s_setprio(G < 4 ? 3 - G : (G & 1) ^ 1);
  }
  asm volatile("" :: "v"(c.f[Q % 3][0]), "v"(c.f[Q % 3][1]), "v"(c.f[Q % 3][2]), "v"(c.f[Q % 3][3]));
  SBAR();
  k3_slot<Q, 0>(c, of); k3_slot<Q, 1>(c, of); k3_slot<Q, 2>(c, of); k3_slot<Q, 3>(c, of); k3_slot<Q, 4>(c, of); k3_slot<Q, 5>(c, of); k3_slot<Q, 6>(c, of); k3_slot<Q, 7>(c, of);
  if constexpr (Q > 35) { asm volatile("" ::: "memory"); k3_out<Q - 36>(c); SBAR(); }
}
template <int Q0, int... Qs> DI void k3_quads(K3C& c, bf16x8 (&of)[8][8], std::integer_sequence<int, Qs...>) { (k3_quad<Q0 + Qs>(c, of), ...); }

struct K4C {
  __amdgpu_buffer_rsrc_t srdG, srdH, srdS, srdD; unsigned voffH, m0base, sofW, sofH, aW, voffSt, sofcs, sofcnt, m0x, flag, chk;
  const LAS char* pW; bf16x8 f[3][4], st[2], cs4, wr[20]; f32x4 o[8][2]; LAS char* csimg; char* ws; int j; float nmr[2], rs[2];
};
template <int I, int HALF> DI void k4_dma1(const K4C& c) { dma16i<((I + 6) & (NSLOT - 1)) * SLOT + HALF * 1024, I * 16384 + HALF * 1024, 2>(c.m0base, c.voffH, c.srdG, c.sofW); }
template <int I> DI void k4_dma(const K4C& c) { k4_dma1<I, 0>(c); k4_dma1<I, 1>(c); }
template <int I, int S_> DI void k4_ldh1(const K4C& c, bf16x8& r) { ld16c<S_ * 1024>(r, c.voffH, c.srdH, c.sofH + I * 4096); }
template <int I> DI void k4_ldh(const K4C& c, bf16x8 (&hf)[4]) { k4_ldh1<I, 0>(c, hf[0]); k4_ldh1<I, 1>(c, hf[1]); k4_ldh1<I, 2>(c, hf[2]); k4_ldh1<I, 3>(c, hf[3]); }
template <int Q, int E> DI void k4_rd(K4C& c) {
  if constexpr (Q < 40) { if constexpr ((Q & 3) == 0 && E == 0) c.pW = slot_ptr(c.aW, (Q >> 2) + 6); c.f[Q % 3][E] = lds_rd(c.pW + ((Q & 3) * 4 + E) * 1024); }
}
constexpr int w5_off(int p) { return (p >> 1) * SLOT + (p & 1) * 1024; }
template <int P> DI void w5_dma(const K4C& c) { dma16i<(P < 14 ? w5_off(P) : 0), (20 + P) * 1024, 2>(P < 14 ? c.m0base : c.m0x, c.voffH, c.srdD, 0); }
template <int P> DI void w5_ld(K4C& c) { ld16c<(P & 3) * 1024>(c.wr[P], c.voffH, c.srdD, (P >> 2) * 4096); }
enum { P4_NONE, P4_H, P4_D, P4_W5D, P4_W5R, P4_ST, P4_FL, P4_CS };
constexpr MemOp k4_op(int z) {
  const int I = z >> 5, X = z & 31;
  if (I == 0) return (X & 3) == 1 ? MemOp{P4_H, 2 + (X >> 4), (X >> 2) & 3} : MemOp{P4_NONE, 0, 0};
  if (X < 16 && (X & 3) == 1 && I < 7) return {P4_H, I + 3, X >> 2};
  if ((X == 19 || X == 27) && I < 9) return I < 3 ? MemOp{P4_D, I + 7, X == 27} : MemOp{P4_W5D, 2 * (I - 3) + (X == 27), 0};
  if (I == 7 && (X == 1 || X == 3)) return {P4_ST, X == 3, 0};
  if (I == 8 && X == 1) return {P4_FL, 0, 0};
  if (I == 9 && X == 1) return {P4_CS, 0, 0};
  if (I == 9 && X == 19) return {P4_W5D, 14, 0};
  if (I >= 7 && (X & 3) == 1 && X > 4) { const int r = (I - 7) * 7 + (X >> 2) - 1; if (r < 20) return {P4_W5R, r, 0}; }
  return {P4_NONE, 0, 0};
}
constexpr int k4_younger(int z, int kind, int a) { int n = 0; for (int y = z - 1; y >= 0; --y) { const MemOp o = k4_op(y); if (o.kind == kind && o.a == a) return n; n += o.kind != P4_NONE; } return n; }
constexpr int k4_wait(int i) { return imin(k4_younger(32 * i + 16, P4_H, i + 1), i == 8 ? k4_younger(32 * i + 16, P4_ST, 1) : 63); }
template <int Q, int M> DI void k4_slot(K4C& c, bf16x8 (&hf)[10][4]) {
  constexpr int I = Q >> 2, dt = 2 * (Q & 3) + (M >> 2), u = (M >> 1) & 1, ks = M & 1;
  if constexpr (I == 0 && ks == 0) asm volatile("v_mfma_f32_16x16x32_bf16 %0, %1, %2, 0" : "=&v"(c.o[dt][u]) : "v"(c.f[Q % 3][2 * (M >> 2)]), "v"(hf[0][u]));
  else MFMA16_ACC(c.o[dt][u], c.f[Q % 3][2 * (M >> 2) + ks], hf[I][2 * ks + u]);
  SBAR();
  if constexpr ((M & 1) == 0) k4_rd<Q + 2, (M >> 1)>(c);
  constexpr MemOp o = k4_op(Q * 8 + M);
  if constexpr (o.kind == P4_H) k4_ldh1<o.a, o.b>(c, hf[o.a][o.b]);
  if constexpr (o.kind == P4_D) k4_dma1<o.a, o.b>(c);
  if constexpr (o.kind == P4_W5D) w5_dma<o.a>(c);
  if constexpr (o.kind == P4_W5R) w5_ld<o.a>(c);
  if constexpr (o.kind == P4_ST) ld16c<o.a * 1024>(c.st[o.a], c.voffSt, c.srdH, Z_STAT);
  if constexpr (o.kind == P4_FL) asm volatile("buffer_load_dword %0, off, %1, %2 sc1" : "=v"(c.flag) : "s"(c.srdS), "s"(c.sofcnt) : "memory");
  if constexpr (o.kind == P4_CS) {
    asm volatile("s_waitcnt vmcnt(%1)" : "+v"(c.flag) : "i"(k4_younger(Q * 8 + M, P4_FL, 0)) : "memory");
    if (__builtin_expect(rfl(c.flag) == 0u, 0)) cnt_wait(c.ws, CNT_C + c.j, 1u);
    ld16c<0>(c.cs4, c.voffH, c.srdS, c.sofcs);
  }
  if constexpr ((Q & 3) == 2 && I < 9) {
    const u32x4 t = __builtin_bit_cast(u32x4, hf[I + 1][M >> 1]);
    if constexpr (M == 0) asm volatile("v_max_u32 %0, %1, %2" : "=v"(c.chk) : "v"(t[0]), "v"(t[1]));
    else asm volatile("v_max3_u32 %0, %0, %1, %2" : "+v"(c.chk) : "v"(t[2 * (M & 1)]), "v"(t[2 * (M & 1) + 1]));
  }
  SBAR();
}
template <int Q> DI void k4_quad(K4C& c, bf16x8 (&hf)[10][4]) {
  constexpr int I = Q >> 2;
  if constexpr ((Q & 3) == 2) {
    if constexpr (I < 9) wait_bar4v<k4_wait(I)>(hf[I + 1][0], hf[I + 1][1], hf[I + 1][2], hf[I + 1][3]);
    if constexpr (I == 8) {
    asm volatile("" : "+v"(c.st[0]), "+v"(c.st[1]) :: "memory");
    if (pending(umax4(c.st[0], umax4(c.st[1], 0u)))) {
      unsigned spins = 0;
      do asm volatile("buffer_load_dwordx4 %0, %2, %3, %4 offen sc1\n\tbuffer_load_dwordx4 %1, %2, %3, %4 offen offset:1024 sc1\n\ts_waitcnt vmcnt(0)" : "+v"(c.st[0]), "+v"(c.st[1]) : "v"(c.voffSt), "s"(c.srdH), "s"(Z_STAT) : "memory");
      while (__any(umax4(c.st[0], umax4(c.st[1], 0u)) == 0xffffffffu) && ++spins < SPIN_CAP);
    }
#pragma unroll
    for (int u = 0; u < 2; ++u) {
      const f32x4 sv = __builtin_bit_cast(f32x4, c.st[u]);
      const float mean = sum1632(sv[0] + sv[2]) * 0.125f;
      const float m2 = sum1632(sv[1] + sv[3] + 80.f * ((sv[0] - mean) * (sv[0] - mean) + (sv[2] - mean) * (sv[2] - mean)));
      const float rs = __builtin_amdgcn_rsqf(m2 * (1.f / D) + EPS);
      c.nmr[u] = -mean * rs; c.rs[u] = rs; PIN(c.nmr[u]); PIN(c.rs[u]);
    }
    SBAR();
    }
  }
  if constexpr ((Q & 3) == 3 && I < 9) { asm volatile("" ::: "memory"); reload4<1024>(c.chk, hf[I + 1][0], hf[I + 1][1], hf[I + 1][2], hf[I + 1][3], c.voffH, c.srdH, c.sofH + (I + 1) * 4096); }
  if constexpr (Q < 34) __builtin_amdgcn_s_setprio(3 - ((Q + 2) & 3));
  asm volatile("" :: "v"(c.f[Q % 3][0]), "v"(c.f[Q % 3][1]), "v"(c.f[Q % 3][2]), "v"(c.f[Q % 3][3]));
  SBAR();
  k4_slot<Q, 0>(c, hf); k4_slot<Q, 1>(c, hf); k4_slot<Q, 2>(c, hf); k4_slot<Q, 3>(c, hf); k4_slot<Q, 4>(c, hf); k4_slot<Q, 5>(c, hf); k4_slot<Q, 6>(c, hf); k4_slot<Q, 7>(c, hf);
}
template <int... Qs> DI void k4_quads(K4C& c, bf16x8 (&hf)[10][4], std::integer_sequence<int, Qs...>) { (k4_quad<Qs>(c, hf), ...); }
#define WR_ALL(c) "+v"(c.wr[0]), "+v"(c.wr[1]), "+v"(c.wr[2]), "+v"(c.wr[3]), "+v"(c.wr[4]), "+v"(c.wr[5]), "+v"(c.wr[6]), "+v"(c.wr[7]), "+v"(c.wr[8]), "+v"(c.wr[9]), \
                  "+v"(c.wr[10]), "+v"(c.wr[11]), "+v"(c.wr[12]), "+v"(c.wr[13]), "+v"(c.wr[14]), "+v"(c.wr[15]), "+v"(c.wr[16]), "+v"(c.wr[17]), "+v"(c.wr[18]), "+v"(c.wr[19])
template <int... Ps> DI void w5_dmas(const K4C& c, std::integer_sequence<int, Ps...>) { (w5_dma<Ps>(c), ...); }
template <int P0, int... Ps> DI void w5_lds(K4C& c, std::integer_sequence<int, Ps...>) { (w5_ld<P0 + Ps>(c), ...); }
DI void k5_la(bf16x8 (&r)[4], unsigned voff, __amdgpu_buffer_rsrc_t srd, unsigned so) {
  ld16c<0>(r[0], voff, srd, so); ld16c<1024>(r[1], voff, srd, so); ld16c<2048>(r[2], voff, srd, so); ld16c<3072>(r[3], voff, srd, so);
}

__global__ __launch_bounds__(512, 2) void k2_main(const u16* __restrict__ x, const u16* __restrict__ cnd, const u16* __restrict__ wq, const u16* __restrict__ kc, const u16* __restrict__ vc, const u16* __restrict__ wo, const u16* __restrict__ wd,
                                                   const u16* __restrict__ wg, const u16* __restrict__ wu, const u16* __restrict__ wmod, char* __restrict__ ws, u16* __restrict__ out) {
  extern __shared__ __attribute__((aligned(16))) char smem[];
  {
    size_t pc; int len;
    asm volatile("s_getpc_b64 %0\nadaln_pc_here:\n\ts_mov_b32 %1, adaln_code_end-adaln_pc_here" : "=s"(pc), "=s"(len));
    asm volatile("s_mov_b32 m0, %0\n\ts_nop 0\n\tbuffer_load_dword %1, %2, 0 offen lds"
                 :: "s"((unsigned)(size_t)(LAS char*)smem + L2_SIDE + 45056), "v"(((blockIdx.x >> 3) & 1) * 32768 + threadIdx.x * 64), "s"(mk_srd((const void*)pc, blockIdx.x >= 104 ? len : 0)));
  }
  SBAR();
  unsigned long long tag;
  bf16x8 of[8][8];
  k2_attention(x, cnd, wq, kc, vc, wo, wd, wg, wu, wmod, ws, smem, tag, of[0]);
  SBAR();
  LAS char* sm = (LAS char*)smem;
  const unsigned sb = (unsigned)(size_t)sm;
  const int tid = threadIdx.x, lane = tid & 63, bid = blockIdx.x;
  const unsigned w = rfl(tid >> 6);
  const int kq = (bid & 7) >> 1, rb = (bid & 1) * 4 + (bid >> 6), nt = (bid >> 3) & 7;
  const int frow = rb * 256 + kq * 64 + w * 8 + (lane & 7), fch = lane >> 3;
  const int g4 = lane >> 4, q16 = lane & 15;
  const int j4 = kq * 7 + nt; const bool act4 = nt < 7 && j4 < 27;
  u32x4 x1[2];
  K4C d;
  {
    K3C c;
    d.srdG = mk_srd(ws + WS_WGU, act4 ? D * F * 4 : 0);
    d.voffH = lane * 16; d.m0base = sb + w * 2048; d.sofW = j4 * 163840 + w * 2048;
    c.srdG = d.srdG; c.m04 = d.m0base; c.sof4 = d.sofW;
    c.voff = lane * 16;
#pragma unroll
    for (int i = 0; i < 3; ++i) { unsigned a = sb + lane * 16 + i * 65536; PIN(a); c.pw[i] = (const LAS char*)(size_t)a; }
    c.srdO = mk_srd(ws + WS_O, S * DQ * 2); c.sofO = ((rb * 8 + w) * 128 + kq * 32) * 2048; c.nt = nt; c.ws = ws; c.voffF = WS_CNT + (CNT_SB + (lane & 7)) * 128;
    c.srdM = mk_srd(ws, WS_H); c.srdX = mk_srd(x, S * D * 2);
    { const int r0 = rb * 256 + kq * 64 + w * 8; c.voffXr[0] = ((r0 + fch) * D + nt * 80 + (lane & 7) * 8) * 2; c.voffXr[1] = lane < 16 ? ((r0 + (lane >> 1)) * D + nt * 80 + 64 + (lane & 1) * 8) * 2 : 0x7ff00000u; }
    c.voffM = tid < 240 ? 4 * (nt * 80 + (tid < 80 ? tid : tid < 160 ? 2 * D + tid - 80 : 3 * D + tid - 160)) : 0x7ff00000u;
    c.voffX = h_off(frow, nt * 10 + fch); c.voffX1 = h_off(frow, nt * 10 + 8 + (fch & 1));
    const int tile = rb * 8 + nt, own = w >> 1;
    const __amdgpu_buffer_rsrc_t srdP = mk_srd(ws + WS_PART, Z_END);
    c.srdP = srdP; c.owner = own == kq; c.pOwn = sm + L_OWN + (((w & 1) * 32 + q16) * RLD + g4 * 4) * 4;
    c.voffP = c.owner ? 0x7ff00000u : ((tile * 4 + own) * 3 + (kq - (kq > own))) * 10240 + (w & 1) * 5120 + ((g4 & 1) * 2 + (q16 >> 3)) * 1280 + (g4 >> 1) * 128 + (q16 & 7) * 16;
    k3_rd<0, 0>(c); k3_rd<0, 1>(c); k3_rd<0, 2>(c); k3_rd<0, 3>(c); k3_rd<1, 0>(c); k3_rd<1, 1>(c); k3_rd<1, 2>(c); k3_rd<1, 3>(c);
    SBAR();
    k3_quads<0>(c, of, std::make_integer_sequence<int, 5>{});
    asm volatile("s_waitcnt vmcnt(0)\n\ts_barrier" ::: "memory");
    if (tid < 8) cnt_add(ws, CNT_O + (bid & ~56) + tid * 8);
    if (bid == 0 && tid == 64) __hip_atomic_store((__attribute__((address_space(1))) unsigned long long*)(ws + WS_EP), tag, __ATOMIC_RELAXED, __HIP_MEMORY_SCOPE_AGENT);
    {
      const __amdgpu_buffer_rsrc_t srdW = mk_srd(ws + WS_WOT, DQ * D * 2); const unsigned so = (nt * 4 + kq) * 163840 + w * 1024, m0 = sb + w * 1024;
      dma16i<w3_off(120), 15 * 8192, 2>(m0, c.voff, srdW, so); dma16i<w3_off(128), 16 * 8192, 2>(m0, c.voff, srdW, so); dma16i<w3_off(136), 17 * 8192, 2>(m0, c.voff, srdW, so);
      dma16i<w3_off(144), 18 * 8192, 2>(m0, c.voff, srdW, so); dma16i<w3_off(152), 19 * 8192, 2>(m0, c.voff, srdW, so);
    }
    asm volatile("buffer_load_dword %0, %1, %2, 0 offen sc1" : "=v"(c.flag) : "v"(c.voffF), "s"(c.srdM) : "memory");
    if (w == 0) cnt_wait(ws, CNT_O + bid, 8u);
    asm volatile("s_barrier" ::: "memory");
    k3_lo<1, 0>(c, of[1][0]); k3_lo<1, 1>(c, of[1][1]); k3_lo<1, 2>(c, of[1][2]); k3_lo<1, 3>(c, of[1][3]);
    k3_lo<1, 4>(c, of[1][4]); k3_lo<1, 5>(c, of[1][5]); k3_lo<1, 6>(c, of[1][6]); k3_lo<1, 7>(c, of[1][7]);
    k3_quads<5>(c, of, std::make_integer_sequence<int, 35>{});
    __builtin_amdgcn_s_setprio(0);
    asm volatile("s_nop 7" ::: "memory"); SBAR();
    k3_out<4>(c);
    asm volatile("s_waitcnt vmcnt(%0)" :: "i"(k3_younger(320, P3_X, 1) + 5) : "memory");
    asm volatile("" : "+v"(c.mp[0]), "+v"(c.mp[1]), "+v"(c.mp[2]), "+v"(c.mp[3]), "+v"(c.mp[4]), "+v"(c.xv[0]), "+v"(c.xv[1]));
    { const float mv = rbf(((c.mp[0] + c.mp[1]) + (c.mp[2] + c.mp[3])) + c.mp[4]); *(LAS float*)(sm + L_MOD + tid * 4) = (tid >= 80 && tid < 160) ? rbf(1.f + mv) : mv; }
    asm volatile("s_waitcnt lgkmcnt(0)\n\ts_barrier" ::: "memory");
    f32x2 base[2][4], gav[2][4], a1v[2][4];
#pragma unroll
    for (int it = 0; it < 2; ++it) {
      const int ch = it ? 8 + (fch & 1) : fch;
      const LAS float* po = (const LAS float*)(sm + L_OWN) + (w * 8 + (lane & 7)) * RLD + ch * 8;
      const LAS float* pm = (const LAS float*)(sm + L_MOD) + ch * 8;
      const f32x4 t[2] = {*(const LAS f32x4*)po, *(const LAS f32x4*)(po + 4)};
      const f32x4 ga[2] = {*(const LAS f32x4*)pm, *(const LAS f32x4*)(pm + 4)}, a1[2] = {*(const LAS f32x4*)(pm + 80), *(const LAS f32x4*)(pm + 84)};
      u32x4 xx = __builtin_bit_cast(u32x4, c.xv[it]);
#pragma unroll
      for (int e = 0; e < 4; ++e) xx[e] = __builtin_amdgcn_ds_bpermute(4 * (it ? (lane & 7) * 2 + (fch & 1) : (lane & 7) * 8 + fch), xx[e]);
#pragma unroll
      for (int e = 0; e < 4; ++e) {
        gav[it][e] = (f32x2){ga[e >> 1][2 * (e & 1)], ga[e >> 1][2 * (e & 1) + 1]}; a1v[it][e] = (f32x2){a1[e >> 1][2 * (e & 1)], a1[e >> 1][2 * (e & 1) + 1]};
        base[it][e] = gav[it][e] * (f32x2){t[e >> 1][2 * (e & 1)], t[e >> 1][2 * (e & 1) + 1]} + (f32x2){bflo(xx[e]), bfhi(xx[e])};
        PIN(base[it][e]); PIN(a1v[it][e]);
      }
    }
    if (own != kq) { asm volatile("s_waitcnt vmcnt(0)" ::: "memory"); if (lane == 0) cnt_add(ws, CNT_P + tile * 4 + own); }
    else if (w == 2 * kq) cnt_wait(ws, CNT_P + tile * 4 + kq, 6u);
    asm volatile("s_barrier" ::: "memory");
    u32x4 pv[2][3];
    {
      const unsigned so = (tile * 4 + kq) * 3 * 10240 + w * 1280, v1 = 1024 + (lane & 15) * 16;
      asm volatile("buffer_load_dwordx4 %0, %6, %8, %9 offen sc1\n\tbuffer_load_dwordx4 %2, %6, %8, %10 offen sc1\n\tbuffer_load_dwordx4 %4, %6, %8, %11 offen sc1\n\t"
                   "buffer_load_dwordx4 %1, %7, %8, %9 offen sc1\n\tbuffer_load_dwordx4 %3, %7, %8, %10 offen sc1\n\tbuffer_load_dwordx4 %5, %7, %8, %11 offen sc1\n\ts_waitcnt vmcnt(0)"
                   : "=&v"(pv[0][0]), "=&v"(pv[1][0]), "=&v"(pv[0][1]), "=&v"(pv[1][1]), "=&v"(pv[0][2]), "=&v"(pv[1][2])
                   : "v"(c.voff), "v"(v1), "s"(srdP), "s"(so), "s"(so + 10240), "s"(so + 20480) : "memory");
    }
#pragma unroll
    for (int it = 0; it < 2; ++it) {
      u32x4 y;
#pragma unroll
      for (int e = 0; e < 4; ++e) {
        const f32x2 sum = ((f32x2){bflo(pv[it][0][e]), bfhi(pv[it][0][e])} + (f32x2){bflo(pv[it][1][e]), bfhi(pv[it][1][e])}) + (f32x2){bflo(pv[it][2][e]), bfhi(pv[it][2][e])};
        const f32x2 r = gav[it][e] * sum + base[it][e];
        x1[it][e] = pk2(r[0], r[1]);
        const f32x2 yy = (f32x2){bflo(x1[it][e]), bfhi(x1[it][e])} * a1v[it][e];
        y[e] = pk2(yy[0], yy[1]);
      }
      st16<16>(nopoison(y), srdP, Z_Y + (it == 0 ? c.voffX : lane < 16 ? c.voffX1 : 0x7fffffffu));
    }
    d.srdS = mk_srd(ws, WS_H); d.srdH = srdP; d.sofH = Z_Y + (rb * 8 + w) * 40 * 1024;
    asm volatile("s_barrier" ::: "memory");
    k4_dma<5>(d); k4_dma<6>(d); k4_dma<7>(d);
    {
      float s0 = 0.f, s1 = 0.f, q0 = 0.f, q1 = 0.f;
#pragma unroll
      for (int e = 0; e < 4; ++e) { s0 += bflo(x1[0][e]) + bfhi(x1[0][e]); s1 += bflo(x1[1][e]) + bfhi(x1[1][e]); }
      const float mu = sum816(lane < 16 ? s0 + s1 : s0) * (1.f / 80.f);
#pragma unroll
      for (int e = 0; e < 4; ++e) { q0 += (bflo(x1[0][e]) - mu) * (bflo(x1[0][e]) - mu) + (bfhi(x1[0][e]) - mu) * (bfhi(x1[0][e]) - mu);
                                    q1 += (bflo(x1[1][e]) - mu) * (bflo(x1[1][e]) - mu) + (bfhi(x1[1][e]) - mu) * (bfhi(x1[1][e]) - mu); }
      const float m2 = sum816(lane < 16 ? q0 + q1 : q0);
      __builtin_amdgcn_raw_buffer_store_b64((u32x2){min(__float_as_uint(mu), 0xfffffffeu), min(__float_as_uint(m2), 0xfffffffeu)}, srdP, lane < 8 ? frow * 64 + nt * 8 : 0x7fffffffu, Z_STAT, 16);
    }
  }
  {
    const int k0 = w < 6 ? 7 * w : 42 + 6 * (w - 6), nk = w < 6 ? 7 : 6;
    d.srdD = mk_srd(ws + WS_WDT + (size_t)(nt * 54 + k0) * 5120, nk * 5120);
    d.m0x = sb + L_W5X + w * 4096;
  }
  if (act4) {
    d.aW = sb + lane * 16;
    d.sofcs = WS_CSBS + j4 * 1024; d.sofcnt = WS_CNT + (CNT_C + j4) * 128; d.voffSt = (rb * 256 + w * 32 + q16) * 64 + g4 * 16; d.csimg = sm + L_CS + w * 4096 + lane * 16; d.ws = ws; d.j = j4;
    bf16x8 hf[10][4];
    k4_ldh<0>(d, hf[0]);
    asm volatile("s_waitcnt vmcnt(0)" : "+v"(hf[0][0]), "+v"(hf[0][1]), "+v"(hf[0][2]), "+v"(hf[0][3]) :: "memory");
    reload4<1024>(umax16(hf[0][0], hf[0][1], hf[0][2], hf[0][3]), hf[0][0], hf[0][1], hf[0][2], hf[0][3], d.voffH, d.srdH, d.sofH);
    k4_ldh<1>(d, hf[1]);
    asm volatile("s_barrier" ::: "memory");
    k4_rd<0, 0>(d); k4_rd<0, 1>(d); k4_rd<0, 2>(d); k4_rd<0, 3>(d); k4_rd<1, 0>(d); k4_rd<1, 1>(d); k4_rd<1, 2>(d); k4_rd<1, 3>(d);
    SBAR();
    k4_quads(d, hf, std::make_integer_sequence<int, 40>{});
    __builtin_amdgcn_s_setprio(0);
    asm volatile("s_waitcnt vmcnt(%1)" : "+v"(d.cs4) : "i"(k4_younger(320, P4_CS, 0)) : "memory"); *(LAS bf16x8*)d.csimg = d.cs4;
    asm volatile("s_nop 7" ::: "memory"); SBAR();
    const float nmr[2] = {d.nmr[0], d.nmr[1]}, rg[2] = {d.rs[0] * -1.4426950408889634f, d.rs[1] * -1.4426950408889634f}, ru[2] = {d.rs[0] * -0.6931471805599453f, d.rs[1] * -0.6931471805599453f};
    const LAS float* cb = (const LAS float*)(sm + L_CS + w * 4096) + g4 * 8;
    const __amdgpu_buffer_rsrc_t srdA = mk_srd(ws + WS_ACT, S * F * 2);
    const unsigned vo = (((rb * 4 + (w >> 1)) * 54 + j4 * 2) * 4 + (w & 1) * 2) * 1024 + lane * 16;
#pragma unroll
    for (int s = 0; s < 2; ++s) {
      u32x4 o[2];
#pragma unroll
      for (int t = 0; t < 2; ++t) {
        const f32x4 csg = *(const LAS f32x4*)(cb + 32 * s + 4 * t), bsg = *(const LAS f32x4*)(cb + 64 + 32 * s + 4 * t);
        const f32x4 csu = *(const LAS f32x4*)(cb + 128 + 32 * s + 4 * t), bsu = *(const LAS f32x4*)(cb + 192 + 32 * s + 4 * t);
#pragma unroll
        for (int u = 0; u < 2; ++u)
#pragma unroll
          for (int e = 0; e < 2; ++e) {
            const f32x2 ag = {d.o[4 * s + t][u][2 * e], d.o[4 * s + t][u][2 * e + 1]}, au = {d.o[4 * s + 2 + t][u][2 * e], d.o[4 * s + 2 + t][u][2 * e + 1]};
            const f32x2 g = ag * rg[u] + ((f32x2){csg[2 * e], csg[2 * e + 1]} * nmr[u] + (f32x2){bsg[2 * e], bsg[2 * e + 1]});
            const f32x2 uu = au * ru[u] + ((f32x2){csu[2 * e], csu[2 * e + 1]} * nmr[u] + (f32x2){bsu[2 * e], bsu[2 * e + 1]});
            const f32x2 den = (f32x2){EX2(g[0]), EX2(g[1])} + 1.f;
            const f32x2 a = (g * uu) * (f32x2){__builtin_amdgcn_rcpf(den[0]), __builtin_amdgcn_rcpf(den[1])};
            o[u][2 * t + e] = pk2(a[0], a[1]);
          }
      }
      st16<16>(o[0], srdA, vo + s * 4096); st16<16>(o[1], srdA, vo + s * 4096 + 1024);
    }
    asm volatile("s_waitcnt vmcnt(0)\n\ts_barrier" : WR_ALL(d) :: "memory");
    if (tid < 32) cnt_add(ws, CNT_A + rb * 32 + tid);
    w5_dma<12>(d); w5_dma<13>(d);
  } else {
    const int jc = (bid >> 6) * 8 + (bid & 7);
    asm volatile("s_waitcnt vmcnt(0)\n\ts_barrier" ::: "memory");
    if (nt == 7 && jc < 27) {
      auto mod = [&](int i) { return __uint_as_float(__builtin_amdgcn_raw_buffer_load_b32(d.srdS, WS_MOD2 + (D + i) * 4, 0, 16)); };
      float mv[3];
#pragma unroll
      for (int i = 0; i < 3; ++i) { const int t = min(tid + 512 * i, 1279); mv[i] = ((mod(t) + mod(2560 + t)) + (mod(5120 + t) + mod(7680 + t))) + mod(10240 + t); }
      const __amdgpu_buffer_rsrc_t srdT = mk_srd(ws + WS_WGU, D * F * 4);
      u32x4 wv[20];
#pragma unroll
      for (int i = 0; i < 20; ++i) wv[i] = __builtin_amdgcn_raw_buffer_load_b128(srdT, lane * 16 + (i & 1) * 1024, jc * 163840 + (i >> 1) * 16384 + w * 2048, 16);
#pragma unroll
      for (int i = 0; i < 3; ++i) { const int t = min(tid + 512 * i, 1279); const float r = rbf(mv[i]); *(LAS u16*)(sm + (t < D ? D + t : t - D) * 2) = (u16)pk2(t < D ? r : 1.f + r, 0.f); }
      asm volatile("s_waitcnt lgkmcnt(0)\n\ts_barrier" ::: "memory");
      f32x4 acc = {};
#pragma unroll
      for (int i = 0; i < 20; ++i) acc = MFMA16(__builtin_bit_cast(bf16x8, wv[i]), lds_rd(sm + (lane & 1) * (D * 2) + g4 * 16 + i * 64), acc);
      acc *= (w & 2) ? -0.6931471805599453f : -1.4426950408889634f;
      st16<16>(__builtin_bit_cast(u32x4, acc), d.srdS, q16 < 2 ? WS_CSBS + jc * 1024 + (((w >> 1) & 1) * 128 + q16 * 64 + (w >> 2) * 32 + g4 * 8 + (w & 1) * 4) * 4 : 0x7fffffffu);
      asm volatile("s_waitcnt vmcnt(0) lgkmcnt(0)\n\ts_barrier" ::: "memory");
      if (tid == 0) cnt_add(ws, CNT_C + jc);
    }
    w5_dmas(d, std::make_integer_sequence<int, 15>{}); w5_lds<0>(d, std::make_integer_sequence<int, 20>{});
    asm volatile("s_waitcnt vmcnt(0)" : WR_ALL(d) :: "memory");
  }
  {
    const int k0 = w < 6 ? 7 * w : 42 + 6 * (w - 6), nk = w < 6 ? 7 : 6;
    const __amdgpu_buffer_rsrc_t srdA = mk_srd(ws + WS_ACT + (size_t)((rb * 4 + kq) * 54 + k0) * 4096, nk * 4096);
    f32x4 gb[2][2];
#pragma unroll
    for (int it = 0; it < 2; ++it) {
      const LAS float* pm = (const LAS float*)(sm + L_MOD) + 160 + (it ? 8 + (lane & 1) : lane & 7) * 8; gb[it][0] = *(const LAS f32x4*)pm; gb[it][1] = *(const LAS f32x4*)(pm + 4);
#pragma unroll
      for (int e = 0; e < 4; ++e) x1[it][e] = __builtin_amdgcn_ds_bpermute(4 * (it ? (lane & 1) * 8 + ((lane >> 1) & 7) : (lane & 7) * 8 + fch), x1[it][e]);
    }
    unsigned fr[2], oo[2];
#pragma unroll
    for (int it = 0; it < 2; ++it) {
      const int ch = it ? 8 + (lane & 1) : lane & 7, r = w * 8 + (it ? q16 >> 1 : fch), rp = r ^ ((ch & 1) * 4);
      fr[it] = sb + (it ? L_OWN + (ch & 1) * 2048 + g4 * 8192 : ch * SLOT) + rp * 32 + (((rp >> 2) ^ (ch >> 1)) & 1) * 16;
      oo[it] = (it == 0 || lane < 16) ? ((rb * 256 + kq * 64 + r) * D + nt * 80 + ch * 8) * 2 : 0x7fffffffu;
      PIN(fr[it]); PIN(oo[it]);
    }
    if (w == 0) cnt_wait(ws, CNT_A + rb * 32 + kq * 8 + nt, 27u);
    asm volatile("s_waitcnt lgkmcnt(0)\n\ts_barrier\n\ts_nop 4" : "+v"(gb[0][0]), "+v"(gb[0][1]), "+v"(gb[1][0]), "+v"(gb[1][1]), "+v"(x1[0]), "+v"(x1[1]) :: "memory");
    bf16x8 fa[7][4];
    k5_la(fa[0], d.voffH, srdA, 0);
    asm volatile("s_barrier" ::: "memory");
    k5_la(fa[1], d.voffH, srdA, 4096); k5_la(fa[2], d.voffH, srdA, 8192);
    f32x4 acc[4][5] = {};
    const LAS char* pw = sm + w * 2048 + lane * 16; const LAS char* pwh; { unsigned a = sb + w * 2048 + lane * 16 + 65536; PIN(a); pwh = (const LAS char*)(size_t)a; }
    const LAS char* pwx = sm + L_W5X + w * 4096 + lane * 16;
    bf16x8 fl[5];
#define K5_STEP(J) do { \
      asm volatile("s_waitcnt vmcnt(%4)" : "+v"(fa[J][0]), "+v"(fa[J][1]), "+v"(fa[J][2]), "+v"(fa[J][3]) : "i"(J < 5 ? 8 : J == 5 ? 4 : 0) : "memory"); \
      _Pragma("unroll") for (int cc = 0; cc < 5; ++cc) { \
        _Pragma("unroll") for (int r = 0; r < 4; ++r) acc[r][cc] = MFMA16(J < 4 ? d.wr[(J < 4 ? J : 0) * 5 + cc] : fl[cc], fa[J][r], acc[r][cc]); \
        if (J >= 3 && J < 6) { const int pp = (J - 3) * 5 + cc; fl[cc] = lds_rd(pp == 14 ? pwx : w5_off(pp) < 65536 ? pw + w5_off(pp) : pwh + (w5_off(pp) - 65536)); } \
        SBAR(); } \
      if (J + 3 < 7) k5_la(fa[J + 3 < 7 ? J + 3 : 0], d.voffH, srdA, (J + 3) * 4096); \
      SBAR(); } while (0)
    K5_STEP(0); K5_STEP(1); K5_STEP(2); K5_STEP(3); K5_STEP(4); K5_STEP(5);
    {
      const unsigned pr_ = q16 ^ ((g4 >> 1) * 4), lo = pr_ * 32 + (((g4 & 1) ^ ((pr_ >> 2) & 1)) * 16);
      unsigned a0 = sb + w * 2048 + (g4 >> 1) * SLOT + lo, a1 = a0 + 65536, a2 = sb + L_OWN + w * 4096 + (g4 >> 1) * 2048 + lo, a0x = a0 ^ 16, a1x = a1 ^ 16;
      PIN(a0); PIN(a1); PIN(a2); PIN(a0x); PIN(a1x);
      asm volatile("s_waitcnt vmcnt(0) lgkmcnt(0)" : "+v"(fa[6][0]), "+v"(fa[6][1]), "+v"(fa[6][2]), "+v"(fa[6][3]), "+v"(fl[0]), "+v"(fl[1]), "+v"(fl[2]), "+v"(fl[3]), "+v"(fl[4]) :: "memory");
#pragma unroll
      for (int cc = 0; cc < 6; ++cc)
#pragma unroll
        for (int r = 0; r < 4; ++r) {
          if (cc < 5) acc[r][cc] = MFMA16(fl[cc], fa[6][r], acc[r][cc]);
          SBAR();
          if (cc > 0) { const int e = cc - 1; *(LAS f32x4*)((LAS char*)(size_t)(e == 0 ? a0 : e == 1 ? a0x : e == 2 ? a1 : e == 3 ? a1x : a2) + (e & 1) * 2 * SLOT + r * 512) = acc[r][e]; }
          SBAR();
        }
    }
    asm volatile("s_waitcnt lgkmcnt(0)\n\ts_barrier" ::: "memory");
    f32x4 t0[2][8], t1[2][8];
#pragma unroll
    for (int it = 1; it >= 0; --it)
#pragma unroll
      for (int i = 0; i < (it ? 2 : 8); ++i) { t0[it][i] = *(const LAS f32x4*)((const LAS char*)(size_t)fr[it] + i * (it ? 4096 : 2048)); t1[it][i] = *(const LAS f32x4*)((const LAS char*)(size_t)(fr[it] ^ 16) + i * (it ? 4096 : 2048)); }
    SBAR();
    f32x4 sm0[2], sm1[2];
    sm0[1] = t0[1][0] + t0[1][1]; sm1[1] = t1[1][0] + t1[1][1];
#pragma unroll
    for (int e = 0; e < 4; ++e) { sm0[1][e] = sum1632(sm0[1][e]); sm1[1][e] = sum1632(sm1[1][e]); }
    sm0[0] = ((t0[0][0] + t0[0][1]) + (t0[0][2] + t0[0][3])) + ((t0[0][4] + t0[0][5]) + (t0[0][6] + t0[0][7]));
    sm1[0] = ((t1[0][0] + t1[0][1]) + (t1[0][2] + t1[0][3])) + ((t1[0][4] + t1[0][5]) + (t1[0][6] + t1[0][7]));
    const __amdgpu_buffer_rsrc_t srdOut = mk_srd(out, S * D * 2);
#pragma unroll
    for (int it = 1; it >= 0; --it) {
      const f32x4 g0 = gb[it][0], g1 = gb[it][1], s0 = sm0[it], s1 = sm1[it];
      u32x4 o;
      o[0] = pk2(bflo(x1[it][0]) + g0[0] * s0[0], bfhi(x1[it][0]) + g0[1] * s0[1]); o[1] = pk2(bflo(x1[it][1]) + g0[2] * s0[2], bfhi(x1[it][1]) + g0[3] * s0[3]);
      o[2] = pk2(bflo(x1[it][2]) + g1[0] * s1[0], bfhi(x1[it][2]) + g1[1] * s1[1]); o[3] = pk2(bflo(x1[it][3]) + g1[2] * s1[2], bfhi(x1[it][3]) + g1[3] * s1[3]);
      st16<16>(o, srdOut, oo[it]);
    }
  }
  asm volatile("adaln_code_end:");
}

extern "C" void kernel_launch(void* const* d_in, const int* in_sizes, int n_in, void* d_out, int out_size, void* d_ws, size_t ws_size, hipStream_t stream) {
  const u16* x = (const u16*)d_in[0]; const u16* kc = (const u16*)d_in[1]; const u16* vc = (const u16*)d_in[2];
  const u16* wq = (const u16*)d_in[3]; const u16* wo = (const u16*)d_in[4];
  const u16* wg = (const u16*)d_in[6]; const u16* wu = (const u16*)d_in[7]; const u16* wd = (const u16*)d_in[8];
  const u16* c = (const u16*)d_in[9]; const u16* wmod = (const u16*)d_in[10];
  char* ws = (char*)d_ws; u16* out = (u16*)d_out;
  (void)hipFuncSetAttribute((const void*)k2_main, hipFuncAttributeMaxDynamicSharedMemorySize, LDS3);
  hipLaunchKernelGGL(k2_main, dim3(256), dim3(512), LDS3, stream, x, c, wq, kc, vc, wo, wd, wg, wu, wmod, ws, out);
}
```
